```python
import torch

def _softmax(x, axis=-1):
    return torch.softmax(x, dim=axis)
def _gelu(x):
    return torch.nn.functional.gelu(x, approximate="tanh")
def _silu(x):
    return torch.nn.functional.silu(x)
def _sigmoid(x):
    return torch.sigmoid(x)
def _layernorm(x, axis=-1, eps=1e-5):
    m = x.mean(dim=axis, keepdim=True)
    v = x.var(dim=axis, keepdim=True, unbiased=False)
    return (x - m) / torch.sqrt(v + eps)
def _scatter_add(tgt, idx, src, axis=0):
    return tgt.clone().index_add_(axis, idx, src)
def _segment_sum(src, seg, n):
    out = torch.zeros((n,) + src.shape[1:], dtype=src.dtype)
    return out.index_add_(0, seg, src)
def _pad(x, pw):
    flat = tuple(v for pair in reversed(pw) for v in pair)
    return torch.nn.functional.pad(x, flat)

kernel_name = 'standalone__sequential_behavior_din__recsys'

def setup_inputs():

    _w = (lambda a: (lambda t: t.to(torch.bfloat16) if t.is_floating_point() else t)(torch.as_tensor(a)))
    B, L, D = 16384, 50, 16
    H = D
    return {
        "target": _w((lambda *s: torch.randn(*s, dtype=torch.bfloat16) * 1.6)(B, D)),
        "hist": _w((lambda *s: torch.randn(*s, dtype=torch.bfloat16) * 1.6)(B, L, D)),
        "mask": _w((lambda lo, hi, *s: torch.randint(lo, hi, s))(0, 2, B, L).to(float)),
        "w1": _w((lambda *s: torch.randn(*s, dtype=torch.bfloat16) * 1.6)(4 * D, H) * (4 * D) ** -0.5),
        "b1": _w((lambda *s: torch.randn(*s, dtype=torch.bfloat16) * 1.6)(H) * 0.1),
        "w2": _w((lambda *s: torch.randn(*s, dtype=torch.bfloat16) * 0.05)(H, 1) * H ** -0.5),
        "b2": _w((lambda *s: torch.randn(*s, dtype=torch.bfloat16) * 0.05)(1) * 0.1),
    }

def reference(target, hist, mask, w1, b1, w2, b2):
    L = hist.shape[1]
    tgt = (lambda x, n, axis: torch.repeat_interleave(x, n, dim=axis))(target[:, None, :], L, axis=1)
    feat = torch.concatenate([hist, tgt, hist - tgt, hist * tgt], axis=-1)
    h = _sigmoid(feat @ w1 + b1)
    scores = (h @ w2 + b2)[..., 0] * mask
    return torch.einsum('bl,bld->bd', scores, hist)
```

```cpp
#include <hip/hip_runtime.h>
#include <hip/hip_bf16.h>
#include <stdint.h>

static constexpr int B_ = 16384, L_ = 50, D_ = 16, H_ = 16;

__device__ __forceinline__ float bf2f(unsigned short u) { return __uint_as_float(((unsigned)u) << 16); }

__global__ __launch_bounds__(256) void din_naive(const unsigned short* __restrict__ target,
                                                 const unsigned short* __restrict__ hist,
                                                 const unsigned short* __restrict__ mask,
                                                 const unsigned short* __restrict__ w1,
                                                 const unsigned short* __restrict__ b1,
                                                 const unsigned short* __restrict__ w2,
                                                 const unsigned short* __restrict__ b2,
                                                 __hip_bfloat16* __restrict__ out) {
    __shared__ float sw1[64 * 16];
    __shared__ float sb1[16], sw2[16];
    for (int i = threadIdx.x; i < 1024; i += 256) sw1[i] = bf2f(w1[i]);
    if (threadIdx.x < 16) { sb1[threadIdx.x] = bf2f(b1[threadIdx.x]); sw2[threadIdx.x] = bf2f(w2[threadIdx.x]); }
    __syncthreads();
    int wave = threadIdx.x >> 6, lane = threadIdx.x & 63;
    int b = blockIdx.x * 4 + wave;
    float t[16], h[16];
    for (int d = 0; d < 16; ++d) t[d] = bf2f(target[b * 16 + d]);
    bool act = lane < L_;
    for (int d = 0; d < 16; ++d) h[d] = act ? bf2f(hist[((size_t)b * L_ + lane) * 16 + d]) : 0.f;
    float m = act ? bf2f(mask[b * L_ + lane]) : 0.f;
    float s = bf2f(b2[0]);
    for (int j = 0; j < 16; ++j) {
        float z = sb1[j];
        for (int d = 0; d < 16; ++d) {
            z += h[d] * sw1[d * 16 + j];
            z += t[d] * sw1[(16 + d) * 16 + j];
            z += (h[d] - t[d]) * sw1[(32 + d) * 16 + j];
            z += (h[d] * t[d]) * sw1[(48 + d) * 16 + j];
        }
        float sg = 1.f / (1.f + __expf(-z));
        s += sg * sw2[j];
    }
    s *= m;
    float res = 0.f;
    for (int d = 0; d < 16; ++d) {
        float v = s * h[d];
        for (int o = 32; o > 0; o >>= 1) v += __shfl_xor(v, o);
        if (lane == d) res = v;
    }
    if (lane < 16) out[b * 16 + lane] = __float2bfloat16(res);
}

extern "C" void kernel_launch(void* const* d_in, const int* in_sizes, int n_in,
                              void* d_out, int out_size, void* d_ws, size_t ws_size,
                              hipStream_t stream) {
    din_naive<<<B_ / 4, 256, 0, stream>>>((const unsigned short*)d_in[0], (const unsigned short*)d_in[1],
                                          (const unsigned short*)d_in[2], (const unsigned short*)d_in[3],
                                          (const unsigned short*)d_in[4], (const unsigned short*)d_in[5],
                                          (const unsigned short*)d_in[6], (__hip_bfloat16*)d_out);
}
```

```cpp
#include <hip/hip_runtime.h>
#include <hip/hip_bf16.h>
#include <stdint.h>

typedef float f32x2 __attribute__((ext_vector_type(2)));
typedef float f32x4 __attribute__((ext_vector_type(4)));
typedef short s16x4 __attribute__((ext_vector_type(4)));
typedef __bf16 bf16x2 __attribute__((ext_vector_type(2)));
typedef __bf16 bf16x8 __attribute__((ext_vector_type(8)));
typedef unsigned u32x2 __attribute__((ext_vector_type(2)));
typedef unsigned u32x4 __attribute__((ext_vector_type(4)));
#define LDSP __attribute__((address_space(3)))
#define DI __device__ __forceinline__

#define PF 3
#define NTS " nt"

static constexpr int WAVES = 4;
static constexpr int SPW = 8;
static constexpr int HB = 1600;
static constexpr int L_HIST = 0;
static constexpr int L_W1 = WAVES * SPW * HB;
static constexpr int L_SM = L_W1 + 2048;
static constexpr int SM = 2048 + 512 + 256;
static constexpr int L_TOTAL = L_SM + WAVES * SM;

DI float lo_f(unsigned u) { return __uint_as_float(u << 16); }
DI float hi_f(unsigned u) { return __uint_as_float(u & 0xffff0000u); }
DI unsigned pk(float a, float b) {
    f32x2 v = {a, b};
    return __builtin_bit_cast(unsigned, __builtin_convertvector(v, bf16x2));
}
DI f32x4 mfma32(u32x4 a, u32x4 b, f32x4 c) {
    return __builtin_amdgcn_mfma_f32_16x16x32_bf16(__builtin_bit_cast(bf16x8, a), __builtin_bit_cast(bf16x8, b), c, 0, 0, 0);
}
DI f32x4 mfma16(u32x2 a, u32x2 b, f32x4 c) {
    return __builtin_amdgcn_mfma_f32_16x16x16bf16_1k(__builtin_bit_cast(s16x4, a), __builtin_bit_cast(s16x4, b), c, 0, 0, 0);
}
DI u32x2 lds64(unsigned a) { return *(const LDSP u32x2*)a; }
DI f32x4 lds128f(unsigned a) { return *(const LDSP f32x4*)a; }
DI f32x2 lds64f(unsigned a) { return *(const LDSP f32x2*)a; }
DI u32x2 ldstr(unsigned a) {
    return __builtin_bit_cast(u32x2, __builtin_amdgcn_ds_read_tr16_b64_v4i16((LDSP s16x4*)a));
}
DI u32x2 sig4(f32x4 z) {
    f32x2 e0 = {__builtin_amdgcn_exp2f(z[0]), __builtin_amdgcn_exp2f(z[1])};
    f32x2 e1 = {__builtin_amdgcn_exp2f(z[2]), __builtin_amdgcn_exp2f(z[3])};
    e0 += 1.0f; e1 += 1.0f;
    u32x2 r;
    r[0] = pk(__builtin_amdgcn_rcpf(e0[0]), __builtin_amdgcn_rcpf(e0[1]));
    r[1] = pk(__builtin_amdgcn_rcpf(e1[0]), __builtin_amdgcn_rcpf(e1[1]));
    return r;
}

template <int N> DI void waitvm() { asm volatile("s_waitcnt vmcnt(%0)" ::"n"(N) : "memory"); }

DI void dma_sample(unsigned voff, const char* sbase, unsigned ldsaddr) {
    asm volatile(
        "s_mov_b32 m0, %2\n\t"
        "s_nop 0\n\t"
        "global_load_lds_dwordx4 %0, %1" NTS "\n\t"
        "s_mov_b32 exec_hi, 15\n\t"
        "global_load_lds_dwordx4 %0, %1 offset:1024" NTS "\n\t"
        "s_mov_b32 exec_hi, -1"
        ::"v"(voff), "s"(sbase), "s"(ldsaddr) : "memory");
}

__global__ __launch_bounds__(256, 2) void din_kernel(const char* __restrict__ target, const char* __restrict__ hist,
                                                     const char* __restrict__ mask, const char* __restrict__ w1,
                                                     const char* __restrict__ b1, const char* __restrict__ w2,
                                                     const char* __restrict__ b2, char* __restrict__ out
    ) {
    __shared__ __attribute__((aligned(16))) char smem[L_TOTAL];
    const unsigned lane = threadIdx.x & 63;
    const unsigned wib = __builtin_amdgcn_readfirstlane(threadIdx.x >> 6);
    const unsigned wave = blockIdx.x * WAVES + wib;
    const unsigned sbase = (unsigned)(size_t)(LDSP char*)smem;
    const unsigned n = lane & 15, g = lane >> 4;

    const unsigned hw = sbase + L_HIST + wib * (SPW * HB);
    const char* hsrc = hist + (size_t)wave * (SPW * HB);
    const unsigned hoff = 16u * (lane ^ ((lane >> 4) & 1));
    dma_sample(hoff, hsrc, hw);

    u32x2 rb1, rw2;
    unsigned rt;
    u32x4 rm = {0, 0, 0, 0};
    {
        const unsigned c = lane & 7, s = lane >> 3;
        const unsigned moff = wave * 800u + 100u * s + (c < 6 ? 16u * c : 84u);
        const unsigned toff = wave * 256u + 4u * lane;
        asm volatile(
            "s_mov_b32 m0, %[lw1]\n\t"
            "s_nop 0\n\t"
            "global_load_lds_dwordx4 %[l16], %[w1]\n\t"
            "global_load_lds_dwordx4 %[l16], %[w1] offset:1024\n\t"
            "global_load_dwordx2 %[rb1], %[g8], %[b1]\n\t"
            "global_load_dwordx2 %[rw2], %[g8], %[w2]\n\t"
            "global_load_dword %[rt], %[toff], %[tg]" NTS "\n\t"
            "s_mov_b32 exec_lo, 0x7f7f7f7f\n\t"
            "s_mov_b32 exec_hi, 0x7f7f7f7f\n\t"
            "global_load_dwordx4 %[rm], %[moff], %[mk]" NTS "\n\t"
            "s_mov_b64 exec, -1"
            : [rb1] "=&v"(rb1), [rw2] "=&v"(rw2), [rt] "=&v"(rt), [rm] "+v"(rm)
            : [lw1] "s"(sbase + L_W1), [l16] "v"(lane * 16u), [w1] "s"(w1), [g8] "v"(g * 8u), [b1] "s"(b1), [w2] "s"(w2),
              [toff] "v"(toff), [tg] "s"(target), [moff] "v"(moff), [mk] "s"(mask)
            : "memory");
    }
#pragma unroll
    for (int s = 1; s < PF; ++s) dma_sample(hoff, hsrc + s * HB, hw + s * HB);

    const float b2f = lo_f(*(const __attribute__((address_space(4))) unsigned*)b2);

    const unsigned sm = sbase + L_SM + wib * SM;
    const unsigned a_row = hw + 32u * n + ((8u * g) ^ (16u * (n >> 3)));
    const unsigned a_tr = hw + ((8u * lane) ^ (16u * (lane >> 5)));
    const unsigned a_tf = sm + 2048 + 16u * g;
    const unsigned a_tb = sm + 2560 + 8u * g;
    unsigned a_m[4];
#pragma unroll
    for (int q = 0; q < 4; ++q) a_m[q] = sm + (((n >> 2) == (unsigned)q && (n & 3) < 3) ? (64u * (n & 3) + 16u * g) : 224u);

    asm volatile("s_waitcnt vmcnt(%4)" : "+v"(rb1), "+v"(rw2), "+v"(rt), "+v"(rm) : "n"(2 * (PF - 1)) : "memory");
    {
        f32x4 m0 = {lo_f(rm[0]), hi_f(rm[0]), lo_f(rm[1]), hi_f(rm[1])};
        f32x4 m1 = {lo_f(rm[2]), hi_f(rm[2]), lo_f(rm[3]), hi_f(rm[3])};
        *(LDSP f32x4*)(sm + 32u * lane) = m0;
        *(LDSP f32x4*)(sm + 32u * lane + 16) = m1;
        f32x2 tf = {lo_f(rt), hi_f(rt)};
        *(LDSP f32x2*)(sm + 2048 + 8u * lane) = tf;
        *(LDSP unsigned*)(sm + 2560 + 4u * lane) = rt;
    }
    const float K = -1.4426950408889634f;
    f32x4 Wac, Wd;
    u32x2 Wbc_b, Wac_b, Wd_b;
    {
        const unsigned aw = sbase + L_W1 + 8u * lane;
        u32x2 wa = ldstr(aw), wb = ldstr(aw + 512), wc = ldstr(aw + 1024), wd = ldstr(aw + 1536);
        f32x4 fa = {lo_f(wa[0]), hi_f(wa[0]), lo_f(wa[1]), hi_f(wa[1])};
        f32x4 fb = {lo_f(wb[0]), hi_f(wb[0]), lo_f(wb[1]), hi_f(wb[1])};
        f32x4 fc = {lo_f(wc[0]), hi_f(wc[0]), lo_f(wc[1]), hi_f(wc[1])};
        f32x4 fd = {lo_f(wd[0]), hi_f(wd[0]), lo_f(wd[1]), hi_f(wd[1])};
        Wac = (fa + fc) * K;
        Wd = fd * K;
        f32x4 Wbc = (fb - fc) * K;
        Wbc_b = (u32x2){pk(Wbc[0], Wbc[1]), pk(Wbc[2], Wbc[3])};
        Wac_b = (u32x2){pk(Wac[0], Wac[1]), pk(Wac[2], Wac[3])};
        Wd_b = (u32x2){pk(Wd[0], Wd[1]), pk(Wd[2], Wd[3])};
    }
    const f32x4 b1s = (f32x4){lo_f(rb1[0]), hi_f(rb1[0]), lo_f(rb1[1]), hi_f(rb1[1])} * K;
    const f32x4 b2s = {b2f, b2f, b2f, b2f};
    const unsigned n3 = n & 3;
    const u32x4 B2a = {n3 == 0 ? rw2[0] : 0u, n3 == 0 ? rw2[1] : 0u, n3 == 1 ? rw2[0] : 0u, n3 == 1 ? rw2[1] : 0u};
    const u32x2 B2b = {n3 == 2 ? rw2[0] : 0u, n3 == 2 ? rw2[1] : 0u};

    float acc[2];
    const f32x4 zero4 = {0.f, 0.f, 0.f, 0.f};
    f32x4 C0 = zero4, C1 = zero4, C2 = zero4;

    auto body = [&](auto SC) {
        constexpr int s = decltype(SC)::value;
        constexpr int q = s & 3;
        constexpr int last = (s + PF - 1 < SPW - 1) ? s + PF - 1 : SPW - 1;
        waitvm<2 * (last - s)>();
        if constexpr (s + PF < SPW) dma_sample(hoff, hsrc + (s + PF) * HB, hw + (s + PF) * HB);
        const f32x4 tf = lds128f(a_tf + 64 * s);
        const u32x2 tb = lds64(a_tb + 32 * s);
        const u32x2 h0 = lds64(a_row + HB * s), h1 = lds64(a_row + HB * s + 512), h2 = lds64(a_row + HB * s + 1024);
        const f32x4 We = Wac + tf * Wd;
        const u32x4 A1 = {pk(We[0], We[1]), pk(We[2], We[3]), Wbc_b[0], Wbc_b[1]};
        const f32x4 z0 = mfma32(A1, (u32x4){h0[0], h0[1], tb[0], tb[1]}, b1s);
        const f32x4 z1 = mfma32(A1, (u32x4){h1[0], h1[1], tb[0], tb[1]}, b1s);
        const f32x4 z2 = mfma32(A1, (u32x4){h2[0], h2[1], tb[0], tb[1]}, b1s);
        const u32x2 s0 = sig4(z0), s1 = sig4(z1), s2 = sig4(z2);
        f32x4 y = mfma32((u32x4){s0[0], s0[1], s1[0], s1[1]}, B2a, b2s);
        y = mfma32((u32x4){s2[0], s2[1], s2[0], s2[1]}, (u32x4){B2b[0], B2b[1], 0u, 0u}, y);
        const f32x4 m = lds128f(a_m[q] + 256 * s);
        y *= m;
        const u32x2 A3 = {pk(y[0], y[1]), pk(y[2], y[3])};
        const u32x2 t0 = ldstr(a_tr + HB * s), t1 = ldstr(a_tr + HB * s + 512), t2 = ldstr(a_tr + HB * s + 1024);
        if constexpr (q == 0) {
            C0 = mfma16(A3, t0, zero4); C1 = mfma16(A3, t1, zero4); C2 = mfma16(A3, t2, zero4);
        } else {
            C0 = mfma16(A3, t0, C0); C1 = mfma16(A3, t1, C1); C2 = mfma16(A3, t2, C2);
        }
        if constexpr (q == 3) acc[s >> 2] = C0[0] + C1[1] + C2[2];
    };
    body(std::integral_constant<int, 0>{});
    body(std::integral_constant<int, 1>{});
    body(std::integral_constant<int, 2>{});
    body(std::integral_constant<int, 3>{});
    body(std::integral_constant<int, 4>{});
    body(std::integral_constant<int, 5>{});
    body(std::integral_constant<int, 6>{});
    body(std::integral_constant<int, 7>{});

    {
        const unsigned sg = n >> 1;
        const u32x2 h = lds64(hw + HB * sg + 1536 + 32u * (n & 1) + 8u * g);
        const u32x2 tb = lds64(a_tb + 32u * sg);
        const f32x4 tf = lds128f(a_tf + 64u * sg);
        const f32x4 hf = {lo_f(h[0]), hi_f(h[0]), lo_f(h[1]), hi_f(h[1])};
        const f32x4 ht = hf * tf;
        f32x4 z = mfma32((u32x4){Wac_b[0], Wac_b[1], Wbc_b[0], Wbc_b[1]}, (u32x4){h[0], h[1], tb[0], tb[1]}, b1s);
        { const unsigned p0 = pk(ht[0], ht[1]), p1 = pk(ht[2], ht[3]);
          z = mfma32((u32x4){Wd_b[0], Wd_b[1], 0u, 0u}, (u32x4){p0, p1, p0, p1}, z); }
        const u32x2 sl = sig4(z);
        f32x4 y = mfma16(sl, rw2, b2s);
        const unsigned sa = 2u * g, sb = 2u * g + 1;
        const f32x2 ma = lds64f(sm + (n == 4u * (sa & 3) + (sa >> 2) ? 256u * sa + 216u : 224u));
        const f32x2 mb = lds64f(sm + (n == 4u * (sb & 3) + (sb >> 2) ? 256u * sb + 216u : 224u));
        const u32x2 A3 = {pk(y[0] * ma[0], y[1] * ma[1]), pk(y[2] * mb[0], y[3] * mb[1])};
        const unsigned k = lane >> 2;
        const u32x2 tl = ldstr(hw + HB * (k >> 1) + 1536 + 32u * (k & 1) + 8u * (lane & 3));
        const f32x4 CL = mfma16(A3, tl, zero4);
        const unsigned o = pk(acc[0] + CL[0], acc[1] + CL[1]);
        char* op = out + (size_t)wave * 256 + 2u * lane;
        *(unsigned short*)op = (unsigned short)o;
        *(unsigned short*)(op + 128) = (unsigned short)(o >> 16);
    }
}

extern "C" void kernel_launch(void* const* d_in, const int* in_sizes, int n_in, void* d_out, int out_size, void* d_ws,
                              size_t ws_size, hipStream_t stream) {
    din_kernel<<<16384 / (WAVES * SPW), WAVES * 64, 0, stream>>>(
        (const char*)d_in[0], (const char*)d_in[1], (const char*)d_in[2], (const char*)d_in[3], (const char*)d_in[4],
        (const char*)d_in[5], (const char*)d_in[6], (char*)d_out
        );
}
```

```cpp
#include <hip/hip_runtime.h>
#include <hip/hip_bf16.h>
#include <stdint.h>

typedef float f32x2 __attribute__((ext_vector_type(2)));
typedef float f32x4 __attribute__((ext_vector_type(4)));
typedef short s16x4 __attribute__((ext_vector_type(4)));
typedef __bf16 bf16x2 __attribute__((ext_vector_type(2)));
typedef __bf16 bf16x8 __attribute__((ext_vector_type(8)));
typedef unsigned u32x2 __attribute__((ext_vector_type(2)));
typedef unsigned u32x4 __attribute__((ext_vector_type(4)));
#define LDSP __attribute__((address_space(3)))
#define DI __device__ __forceinline__

#define PF 8
#define NTS " nt"

static constexpr int WAVES = 4;
static constexpr int SPW = 8;
static constexpr int HB = 1600;
static constexpr int L_HIST = 0;
static constexpr int L_W1 = WAVES * SPW * HB;
static constexpr int L_SM = L_W1 + 2048;
static constexpr int SM = 2048 + 512 + 256;
static constexpr int L_TOTAL = L_SM + WAVES * SM;

DI float lo_f(unsigned u) { return __uint_as_float(u << 16); }
DI float hi_f(unsigned u) { return __uint_as_float(u & 0xffff0000u); }
DI unsigned pk(float a, float b) {
    f32x2 v = {a, b};
    return __builtin_bit_cast(unsigned, __builtin_convertvector(v, bf16x2));
}
DI f32x4 mfma32(u32x4 a, u32x4 b, f32x4 c) {
    return __builtin_amdgcn_mfma_f32_16x16x32_bf16(__builtin_bit_cast(bf16x8, a), __builtin_bit_cast(bf16x8, b), c, 0, 0, 0);
}
DI f32x4 mfma16(u32x2 a, u32x2 b, f32x4 c) {
    return __builtin_amdgcn_mfma_f32_16x16x16bf16_1k(__builtin_bit_cast(s16x4, a), __builtin_bit_cast(s16x4, b), c, 0, 0, 0);
}
DI u32x2 lds64(unsigned a) { return *(const LDSP u32x2*)a; }
DI f32x4 lds128f(unsigned a) { return *(const LDSP f32x4*)a; }
DI f32x2 lds64f(unsigned a) { return *(const LDSP f32x2*)a; }
DI u32x2 ldstr(unsigned a) {
    return __builtin_bit_cast(u32x2, __builtin_amdgcn_ds_read_tr16_b64_v4i16((LDSP s16x4*)a));
}
DI u32x2 sig4(f32x4 z) {
    f32x2 e0 = {__builtin_amdgcn_exp2f(z[0]), __builtin_amdgcn_exp2f(z[1])};
    f32x2 e1 = {__builtin_amdgcn_exp2f(z[2]), __builtin_amdgcn_exp2f(z[3])};
    e0 += 1.0f; e1 += 1.0f;
    u32x2 r;
    r[0] = pk(__builtin_amdgcn_rcpf(e0[0]), __builtin_amdgcn_rcpf(e0[1]));
    r[1] = pk(__builtin_amdgcn_rcpf(e1[0]), __builtin_amdgcn_rcpf(e1[1]));
    return r;
}

template <int N> DI void waitvm() { asm volatile("s_waitcnt vmcnt(%0)" ::"n"(N) : "memory"); }

DI void dma_sample(unsigned voff, const char* sbase, unsigned ldsaddr) {
    asm volatile(
        "s_mov_b32 m0, %2\n\t"
        "s_nop 0\n\t"
        "global_load_lds_dwordx4 %0, %1" NTS "\n\t"
        "s_mov_b32 exec_hi, 15\n\t"
        "global_load_lds_dwordx4 %0, %1 offset:1024" NTS "\n\t"
        "s_mov_b32 exec_hi, -1"
        ::"v"(voff), "s"(sbase), "s"(ldsaddr) : "memory");
}

__global__ __launch_bounds__(256, 2) void din_kernel(const char* __restrict__ target, const char* __restrict__ hist,
                                                     const char* __restrict__ mask, const char* __restrict__ w1,
                                                     const char* __restrict__ b1, const char* __restrict__ w2,
                                                     const char* __restrict__ b2, char* __restrict__ out
    ) {
    unsigned long long T0;
    __shared__ __attribute__((aligned(16))) char smem[L_TOTAL];
    asm volatile("s_memrealtime %0\n s_waitcnt lgkmcnt(0)" : "=s"(T0) :: "memory");
    const unsigned lane = threadIdx.x & 63;
    const unsigned wib = __builtin_amdgcn_readfirstlane(threadIdx.x >> 6);
    const unsigned wave = blockIdx.x * WAVES + wib;
    const unsigned sbase = (unsigned)(size_t)(LDSP char*)smem;
    const unsigned n = lane & 15, g = lane >> 4;

    const unsigned hw = sbase + L_HIST + wib * (SPW * HB);
    const char* hsrc = hist + (size_t)wave * (SPW * HB);
    const unsigned hoff = 16u * (lane ^ ((lane >> 4) & 1));
    dma_sample(hoff, hsrc, hw);

    u32x2 rb1, rw2;
    unsigned rt;
    u32x4 rm = {0, 0, 0, 0};
    {
        const unsigned c = lane & 7, s = lane >> 3;
        const unsigned moff = wave * 800u + 100u * s + (c < 6 ? 16u * c : 84u);
        const unsigned toff = wave * 256u + 4u * lane;
        asm volatile(
            "s_mov_b32 m0, %[lw1]\n\t"
            "s_nop 0\n\t"
            "global_load_lds_dwordx4 %[l16], %[w1]\n\t"
            "global_load_lds_dwordx4 %[l16], %[w1] offset:1024\n\t"
            "global_load_dwordx2 %[rb1], %[g8], %[b1]\n\t"
            "global_load_dwordx2 %[rw2], %[g8], %[w2]\n\t"
            "global_load_dword %[rt], %[toff], %[tg]" NTS "\n\t"
            "s_mov_b32 exec_lo, 0x7f7f7f7f\n\t"
            "s_mov_b32 exec_hi, 0x7f7f7f7f\n\t"
            "global_load_dwordx4 %[rm], %[moff], %[mk]" NTS "\n\t"
            "s_mov_b64 exec, -1"
            : [rb1] "=&v"(rb1), [rw2] "=&v"(rw2), [rt] "=&v"(rt), [rm] "+v"(rm)
            : [lw1] "s"(sbase + L_W1), [l16] "v"(lane * 16u), [w1] "s"(w1), [g8] "v"(g * 8u), [b1] "s"(b1), [w2] "s"(w2),
              [toff] "v"(toff), [tg] "s"(target), [moff] "v"(moff), [mk] "s"(mask)
            : "memory");
    }
#pragma unroll
    for (int s = 1; s < PF; ++s) dma_sample(hoff, hsrc + s * HB, hw + s * HB);

    const float b2f = lo_f(*(const __attribute__((address_space(4))) unsigned*)b2);

    const unsigned sm = sbase + L_SM + wib * SM;
    const unsigned a_row = hw + 32u * n + ((8u * g) ^ (16u * (n >> 3)));
    const unsigned a_tr = hw + ((8u * lane) ^ (16u * (lane >> 5)));
    const unsigned a_tf = sm + 2048 + 16u * g;
    const unsigned a_tb = sm + 2560 + 8u * g;
    unsigned a_m[4];
#pragma unroll
    for (int q = 0; q < 4; ++q) a_m[q] = sm + (((n >> 2) == (unsigned)q && (n & 3) < 3) ? (64u * (n & 3) + 16u * g) : 224u);

    asm volatile("s_waitcnt vmcnt(%4)" : "+v"(rb1), "+v"(rw2), "+v"(rt), "+v"(rm) : "n"(2 * (PF - 1)) : "memory");
    {
        f32x4 m0 = {lo_f(rm[0]), hi_f(rm[0]), lo_f(rm[1]), hi_f(rm[1])};
        f32x4 m1 = {lo_f(rm[2]), hi_f(rm[2]), lo_f(rm[3]), hi_f(rm[3])};
        *(LDSP f32x4*)(sm + 32u * lane) = m0;
        *(LDSP f32x4*)(sm + 32u * lane + 16) = m1;
        f32x2 tf = {lo_f(rt), hi_f(rt)};
        *(LDSP f32x2*)(sm + 2048 + 8u * lane) = tf;
        *(LDSP unsigned*)(sm + 2560 + 4u * lane) = rt;
    }
    const float K = -1.4426950408889634f;
    f32x4 Wac, Wd;
    u32x2 Wbc_b, Wac_b, Wd_b;
    {
        const unsigned aw = sbase + L_W1 + 8u * lane;
        u32x2 wa = ldstr(aw), wb = ldstr(aw + 512), wc = ldstr(aw + 1024), wd = ldstr(aw + 1536);
        f32x4 fa = {lo_f(wa[0]), hi_f(wa[0]), lo_f(wa[1]), hi_f(wa[1])};
        f32x4 fb = {lo_f(wb[0]), hi_f(wb[0]), lo_f(wb[1]), hi_f(wb[1])};
        f32x4 fc = {lo_f(wc[0]), hi_f(wc[0]), lo_f(wc[1]), hi_f(wc[1])};
        f32x4 fd = {lo_f(wd[0]), hi_f(wd[0]), lo_f(wd[1]), hi_f(wd[1])};
        Wac = (fa + fc) * K;
        Wd = fd * K;
        f32x4 Wbc = (fb - fc) * K;
        Wbc_b = (u32x2){pk(Wbc[0], Wbc[1]), pk(Wbc[2], Wbc[3])};
        Wac_b = (u32x2){pk(Wac[0], Wac[1]), pk(Wac[2], Wac[3])};
        Wd_b = (u32x2){pk(Wd[0], Wd[1]), pk(Wd[2], Wd[3])};
    }
    const f32x4 b1s = (f32x4){lo_f(rb1[0]), hi_f(rb1[0]), lo_f(rb1[1]), hi_f(rb1[1])} * K;
    const f32x4 b2s = {b2f, b2f, b2f, b2f};
    const unsigned n3 = n & 3;
    const u32x4 B2a = {n3 == 0 ? rw2[0] : 0u, n3 == 0 ? rw2[1] : 0u, n3 == 1 ? rw2[0] : 0u, n3 == 1 ? rw2[1] : 0u};
    const u32x2 B2b = {n3 == 2 ? rw2[0] : 0u, n3 == 2 ? rw2[1] : 0u};

    float acc[2];
    const f32x4 zero4 = {0.f, 0.f, 0.f, 0.f};
    f32x4 C0 = zero4, C1 = zero4, C2 = zero4;

    auto body = [&](auto SC) {
        constexpr int s = decltype(SC)::value;
        constexpr int q = s & 3;
        constexpr int last = (s + PF - 1 < SPW - 1) ? s + PF - 1 : SPW - 1;
        waitvm<2 * (last - s)>();
        if constexpr (s + PF < SPW) dma_sample(hoff, hsrc + (s + PF) * HB, hw + (s + PF) * HB);
        const f32x4 tf = lds128f(a_tf + 64 * s);
        const u32x2 tb = lds64(a_tb + 32 * s);
        const u32x2 h0 = lds64(a_row + HB * s), h1 = lds64(a_row + HB * s + 512), h2 = lds64(a_row + HB * s + 1024);
        const f32x4 We = Wac + tf * Wd;
        const u32x4 A1 = {pk(We[0], We[1]), pk(We[2], We[3]), Wbc_b[0], Wbc_b[1]};
        const f32x4 z0 = mfma32(A1, (u32x4){h0[0], h0[1], tb[0], tb[1]}, b1s);
        const f32x4 z1 = mfma32(A1, (u32x4){h1[0], h1[1], tb[0], tb[1]}, b1s);
        const f32x4 z2 = mfma32(A1, (u32x4){h2[0], h2[1], tb[0], tb[1]}, b1s);
        const u32x2 s0 = sig4(z0), s1 = sig4(z1), s2 = sig4(z2);
        f32x4 y = mfma32((u32x4){s0[0], s0[1], s1[0], s1[1]}, B2a, b2s);
        y = mfma32((u32x4){s2[0], s2[1], s2[0], s2[1]}, (u32x4){B2b[0], B2b[1], 0u, 0u}, y);
        const f32x4 m = lds128f(a_m[q] + 256 * s);
        y *= m;
        const u32x2 A3 = {pk(y[0], y[1]), pk(y[2], y[3])};
        const u32x2 t0 = ldstr(a_tr + HB * s), t1 = ldstr(a_tr + HB * s + 512), t2 = ldstr(a_tr + HB * s + 1024);
        if constexpr (q == 0) {
            C0 = mfma16(A3, t0, zero4); C1 = mfma16(A3, t1, zero4); C2 = mfma16(A3, t2, zero4);
        } else {
            C0 = mfma16(A3, t0, C0); C1 = mfma16(A3, t1, C1); C2 = mfma16(A3, t2, C2);
        }
        if constexpr (q == 3) acc[s >> 2] = C0[0] + C1[1] + C2[2];
    };
    body(std::integral_constant<int, 0>{});
    body(std::integral_constant<int, 1>{});
    body(std::integral_constant<int, 2>{});
    body(std::integral_constant<int, 3>{});
    body(std::integral_constant<int, 4>{});
    body(std::integral_constant<int, 5>{});
    body(std::integral_constant<int, 6>{});
    body(std::integral_constant<int, 7>{});

    {
        const unsigned sg = n >> 1;
        const u32x2 h = lds64(hw + HB * sg + 1536 + 32u * (n & 1) + 8u * g);
        const u32x2 tb = lds64(a_tb + 32u * sg);
        const f32x4 tf = lds128f(a_tf + 64u * sg);
        const f32x4 hf = {lo_f(h[0]), hi_f(h[0]), lo_f(h[1]), hi_f(h[1])};
        const f32x4 ht = hf * tf;
        f32x4 z = mfma32((u32x4){Wac_b[0], Wac_b[1], Wbc_b[0], Wbc_b[1]}, (u32x4){h[0], h[1], tb[0], tb[1]}, b1s);
        { const unsigned p0 = pk(ht[0], ht[1]), p1 = pk(ht[2], ht[3]);
          z = mfma32((u32x4){Wd_b[0], Wd_b[1], 0u, 0u}, (u32x4){p0, p1, p0, p1}, z); }
        const u32x2 sl = sig4(z);
        f32x4 y = mfma16(sl, rw2, b2s);
        const unsigned sa = 2u * g, sb = 2u * g + 1;
        const f32x2 ma = lds64f(sm + (n == 4u * (sa & 3) + (sa >> 2) ? 256u * sa + 216u : 224u));
        const f32x2 mb = lds64f(sm + (n == 4u * (sb & 3) + (sb >> 2) ? 256u * sb + 216u : 224u));
        const u32x2 A3 = {pk(y[0] * ma[0], y[1] * ma[1]), pk(y[2] * mb[0], y[3] * mb[1])};
        const unsigned k = lane >> 2;
        const u32x2 tl = ldstr(hw + HB * (k >> 1) + 1536 + 32u * (k & 1) + 8u * (lane & 3));
        const f32x4 CL = mfma16(A3, tl, zero4);
        const unsigned o = pk(acc[0] + CL[0], acc[1] + CL[1]);
        char* op = out + (size_t)wave * 256 + 2u * lane;
        *(unsigned short*)op = (unsigned short)o;
        *(unsigned short*)(op + 128) = (unsigned short)(o >> 16);
    }
    while (__builtin_amdgcn_s_memrealtime() - T0 < 2500ull) __builtin_amdgcn_s_sleep(2);
}

extern "C" void kernel_launch(void* const* d_in, const int* in_sizes, int n_in, void* d_out, int out_size, void* d_ws,
                              size_t ws_size, hipStream_t stream) {
    din_kernel<<<16384 / (WAVES * SPW), WAVES * 64, 0, stream>>>(
        (const char*)d_in[0], (const char*)d_in[1], (const char*)d_in[2], (const char*)d_in[3], (const char*)d_in[4],
        (const char*)d_in[5], (const char*)d_in[6], (char*)d_out
        );
}
```

```cpp
#include <hip/hip_runtime.h>

#define LDSP __attribute__((address_space(3)))

__global__ __launch_bounds__(576, 1) __attribute__((aligned(4096))) void din_kernel(const void* target, const void* hist, const void* mask,
                                                     const void* w1, const void* b1, const void* w2,
                                                     const void* b2, void* out) {
    __shared__ __attribute__((aligned(16))) char smem[131392];
    asm volatile(
        "s_load_dwordx16 s[16:31], %0, 0x0\n"
        "v_readfirstlane_b32 s34, %2\n"
        "s_add_u32 s96, %3, 116736\n"
        "s_setprio 3\n"
        "s_lshr_b32 s34, s34, 6\n"
        "s_cbranch_scc0 HELP%=\n"
        "v_and_b32 v1, 63, %2\n"
        "s_add_u32 s35, s34, -1\n"
        "v_bfe_u32 v2, %2, 4, 1\n"
        "s_lshl3_add_u32 s49, %1, s35\n"
        "v_and_b32 v3, 15, %2\n"
        "s_mul_i32 s37, s49, 12800\n"
        "v_lshrrev_b32 v4, 4, v1\n"
        "s_mul_i32 s36, s35, 14592\n"
        "v_xor_b32 v5, v1, v2\n"
        "s_add_u32 s36, s36, %3\n"
        "v_lshlrev_b32 v6, 3, v4\n"
        "s_lshl_b32 s38, s49, 8\n"
        "v_lshl_add_u32 v7, v5, 4, s37\n"
        "s_mov_b32 m0, s36\n"
        "v_lshrrev_b32 v8, 1, v3\n"
        "v_lshrrev_b32 v9, 1, v4\n"
        "v_lshl_add_u32 v5, v8, 5, v6\n"
        "v_add_u32 v2, s38, v5\n"
        "v_lshrrev_b32 v10, 3, v1\n"
        "s_movk_i32 s49, 1536\n"
        "v_lshl_add_u32 v10, v10, 1, 1\n"
        "v_mul_u32_u24 v10, s49, v10\n"
        "s_mov_b32 exec_lo, 0\n"
        "v_mov_b32 v10, -512\n"
        "s_mov_b32 exec_lo, -1\n"
        "v_add_u32 v11, v7, v10\n"
        "v_add_u32 v12, 128, v7\n"
        "v_and_b32 v13, 1, v3\n"
        "v_sub_u32 v14, 0, v13\n"
        "v_lshrrev_b32 v10, 2, v3\n"
        "v_mov_b32 v15, 65535\n"
        "v_mov_b32 v16, 0xffff0000\n"
        "v_cmp_eq_u32 vcc, v10, v4\n"
        "v_lshl_add_u32 v17, v3, 3, s36\n"
        "s_add_u32 s49, s96, -11264\n"
        "v_mov_b32 v18, s49\n"
        "v_cndmask_b32 v19, v18, v17, vcc\n"
        "v_mov_b32 v22, 0\n"
        "v_mov_b32 v23, 0\n"
        "s_mov_b64 exec, 0xffff\n"
        "v_mov_b32 v22, 0xbfb9\n"
        "s_mov_b32 exec_lo, 0xffff0000\n"
        "v_mov_b32 v22, 0x3b2c\n"
        "s_mov_b64 exec, -1\n"
        "v_mov_b32 v27, 0\n"
        "v_mov_b32 v26, v22\n"
        "v_lshl_add_u32 v10, v1, 4, s96\n"
        "s_waitcnt lgkmcnt(0)\n"
        "global_load_dwordx2 v[20:21], v2, s[16:17] nt\n"
        "global_load_lds_dwordx4 v11, s[18:19] nt\n"
        "s_setprio 2\n"
        "s_movk_i32 s48, 160\n"
        "v_lshl_add_u32 v5, v4, 5, s36\n"
        "v_lshl_add_u32 v5, v13, 4, v5\n"
        "v_mad_u32_u24 v18, v8, s48, v5\n"
        "v_add_u32 v28, -1, v13\n"
        "v_lshrrev_b32 v5, 3, v3\n"
        "v_lshlrev_b32 v5, 4, v5\n"
        "v_xor_b32 v5, v5, v6\n"
        "v_lshl_add_u32 v5, v3, 5, v5\n"
        "v_add_u32 v2, s36, v5\n"
        "v_lshlrev_b32 v5, 3, v1\n"
        "v_lshlrev_b32 v17, 4, v9\n"
        "v_xor_b32 v5, v5, v17\n"
        "v_add_u32 v11, s36, v5\n"
        "s_sleep 3\n"
        "s_add_u32 m0, s36, 512\n"
        "s_nop 0\n"
        "global_load_lds_dwordx4 v7, s[18:19] offset:512 nt\n"
        "s_setprio 1\n"
        "v_lshl_add_u32 v29, v4, 4, s36\n"
        "v_lshl_add_u32 v9, v8, 6, v29\n"
        "s_add_u32 s49, s96, 2048\n"
        "v_lshl_add_u32 v5, v4, 8, s49\n"
        "v_lshl_add_u32 v17, v3, 3, v5\n"
        "s_lshr_b32 s49, s37, 4\n"
        "s_and_b32 s47, s49, 127\n"
        "s_sub_u32 s49, s49, s47\n"
        "v_lshl_add_u32 v30, v1, 4, s49\n"
        "s_mul_i32 s39, s35, 896\n"
        "s_add_u32 s39, s39, s96\n"
        "s_add_u32 s39, s39, 4352\n"
        "s_add_u32 s47, s47, s39\n"
        "v_lshl_add_u32 v8, v1, 1, s38\n"
        "s_sleep 1\n"
        "global_load_lds_dwordx4 v12, s[18:19] offset:1536 nt\n"
        "s_setprio 0\n"
        "v_lshrrev_b32 v31, 2, v3\n"
        "v_and_b32 v32, 2, v3\n"
        "v_lshl_add_u32 v33, v32, 1, v31\n"
        "v_and_b32 v34, 1, v31\n"
        "v_mul_u32_u24 v31, 100, v33\n"
        "v_add_u32 v31, s47, v31\n"
        "v_lshl_add_u32 v31, v34, 2, v31\n"
        "v_xad_u32 v32, v34, 1, v13\n"
        "v_lshl_add_u32 v32, v32, 5, v6\n"
        "v_add_u32 v1, v31, v32\n"
        "v_cmp_eq_u32 vcc, 1, v13\n"
        "v_lshl_add_u32 v32, v34, 6, v6\n"
        "v_mul_u32_u24 v3, 104, v34\n"
        "v_sub_u32 v3, 96, v3\n"
        "s_sleep 1\n"
        "global_load_lds_dwordx4 v12, s[18:19] offset:2560 nt\n"
        "s_setprio 3\n"
        "v_cndmask_b32 v32, v32, v3, vcc\n"
        "v_add_u32 v13, v31, v32\n"
        "v_lshrrev_b32 v5, 1, v33\n"
        "v_xor_b32 v5, v5, v4\n"
        "v_lshl_add_u32 v5, v5, 1, v34\n"
        "v_cmp_eq_u32 s[40:41], 0, v5\n"
        "v_cmp_eq_u32 s[42:43], 1, v5\n"
        "s_nop 0\n"
        "v_cndmask_b32 v36, v28, -1, s[40:41]\n"
        "v_cndmask_b32 v37, v28, -1, s[42:43]\n"
        "s_barrier\n"
        "s_branch C2%=\n"
        "HELP%=:\n"
        "s_getpc_b64 s[98:99]\n"
        "CP%=:\n"
        "v_lshlrev_b32 v1, 7, %2\n"
        "v_min_u32 v1, CE%=-CP%=-4, v1\n"
        "global_load_dword v1, v1, s[98:99]\n"
        "v_lshrrev_b32 v2, 4, %2\n"
        "v_and_b32 v3, 15, %2\n"
        "v_lshlrev_b32 v4, 3, v2\n"
        "v_mov_b32 v6, 0\n"
        "v_lshl_add_u32 v5, v3, 5, v4\n"
        "v_lshlrev_b32 v10, 1, v3\n"
        "v_and_b32 v11, 1, %2\n"
        "v_sub_u32 v12, 0, v11\n"
        "v_lshrrev_b32 v13, 2, v3\n"
        "v_xor_b32 v13, v13, v2\n"
        "v_bfe_u32 v14, v3, 1, 1\n"
        "v_lshl_add_u32 v13, v13, 1, v14\n"
        "v_xor_b32 v15, 65535, v12\n"
        "v_cmp_eq_u32 vcc, 0, v13\n"
        "v_cmp_eq_u32 s[52:53], 1, v13\n"
        "v_lshl_add_u32 v11, %2, 4, s96\n"
        "v_cndmask_b32 v16, 0, v15, vcc\n"
        "v_cndmask_b32 v17, 0, v15, s[52:53]\n"
        "s_waitcnt lgkmcnt(0)\n"
        "global_load_dwordx2 v[20:21], v5, s[22:23] offset:1024\n"
        "global_load_dwordx2 v[22:23], v5, s[22:23] offset:512\n"
        "global_load_dwordx2 v[18:19], v5, s[22:23]\n"
        "global_load_dwordx2 v[24:25], v5, s[22:23] offset:1536\n"
        "global_load_ushort v28, v10, s[24:25]\n"
        "global_load_dwordx2 v[30:31], v4, s[26:27]\n"
        "global_load_ushort v3, v6, s[28:29]\n"
        "s_mul_i32 s49, %1, 6400\n"
        "v_mov_b32 v15, s49\n"
        "s_lshl_b32 s49, %1, 11\n"
        "v_mov_b32 v32, s49\n"
        "s_mov_b64 exec, 1\n"
        "global_load_dword v15, v15, s[20:21] nt\n"
        "global_load_dword v32, v32, s[30:31] nt\n"
        "s_mov_b64 exec, -1\n"
        "v_and_b32 v34, 0x3fb93fb9, v16\n"
        "v_and_b32 v35, 0x3fb93fb9, v17\n"
        "v_and_b32 v36, 0xbfb9bfb9, v16\n"
        "v_and_b32 v37, 0xbfb9bfb9, v17\n"
        "v_and_b32 v38, 0xbfb9bfb9, v16\n"
        "v_and_b32 v39, 0xbfb9bfb9, v17\n"
        "v_and_b32 v40, 0xbb2cbb2c, v16\n"
        "v_and_b32 v41, 0xbb2cbb2c, v17\n"
        "v_and_b32 v42, 0x3b2c3b2c, v16\n"
        "v_and_b32 v43, 0x3b2c3b2c, v17\n"
        "v_and_b32 v44, 0x3b2c3b2c, v16\n"
        "v_and_b32 v45, 0x3b2c3b2c, v17\n"
        "v_mov_b32 v7, 0\n"
        "v_mov_b32 v8, 0\n"
        "v_mov_b32 v9, 0\n"
        "v_mov_b32 v29, 0\n"
        "ds_write_b128 v11, v[6:9] offset:2048\n"
        "ds_write_b128 v11, v[6:9] offset:2304\n"
        "ds_write_b128 v11, v[6:9] offset:3328\n"
        "v_lshl_add_u32 v4, v2, 8, s96\n"
        "v_mov_b32 v5, 1\n"
        "s_waitcnt vmcnt(7)\n"
        "v_mfma_f32_16x16x32_bf16 v[46:49], v[20:23], v[34:37], 0\n"
        "v_mfma_f32_16x16x32_bf16 v[46:49], v[20:23], v[40:43], v[46:49]\n"
        "s_waitcnt vmcnt(6)\n"
        "v_mfma_f32_16x16x32_bf16 v[50:53], v[18:21], v[36:39], 0\n"
        "v_mfma_f32_16x16x32_bf16 v[50:53], v[18:21], v[42:45], v[50:53]\n"
        "s_waitcnt vmcnt(5)\n"
        "v_mfma_f32_16x16x16_bf16 v[54:57], v[24:25], v[36:37], 0\n"
        "v_mfma_f32_16x16x16_bf16 v[54:57], v[24:25], v[42:43], v[54:57]\n"
        "s_nop 1\n"
        "v_cvt_pk_bf16_f32 v26, v46, v47\n"
        "v_cvt_pk_bf16_f32 v27, v48, v49\n"
        "s_waitcnt vmcnt(4)\n"
        "ds_write_b128 v11, v[26:29] offset:13632\n"
        "ds_write_b128 v11, v[50:53] offset:11584\n"
        "v_cvt_pk_bf16_f32 v6, v50, v51\n"
        "v_cvt_pk_bf16_f32 v7, v52, v53\n"
        "v_cvt_pk_bf16_f32 v8, v54, v55\n"
        "v_cvt_pk_bf16_f32 v9, v56, v57\n"
        "ds_write_b128 v11, v[6:9] offset:12608\n"
        "s_waitcnt lgkmcnt(0)\n"
        "s_barrier\n"
        "s_waitcnt vmcnt(3)\n"
        "v_and_b32 v13, 0x7fff7fff, v30\n"
        "v_and_b32 v14, 0x7fff7fff, v31\n"
        "s_waitcnt vmcnt(2)\n"
        "v_readfirstlane_b32 s48, v3\n"
        "v_pk_max_u16 v13, v13, v14\n"
        "v_lshlrev_b32 v14, 16, v13\n"
        "v_max_u32 v13, v13, v14\n"
        "s_nop 0\n"
        "v_readlane_b32 s40, v13, 0\n"
        "v_readlane_b32 s41, v13, 16\n"
        "v_readlane_b32 s42, v13, 32\n"
        "v_readlane_b32 s43, v13, 48\n"
        "s_max_u32 s40, s40, s41\n"
        "s_max_u32 s42, s42, s43\n"
        "s_max_u32 s50, s40, s42\n"
        "s_lshr_b32 s50, s50, 23\n"
        "s_bfe_u32 s49, s48, 0x80007\n"
        "s_sub_i32 s49, s49, 30\n"
        "s_max_i32 s50, s50, s49\n"
        "s_max_i32 s50, s50, 15\n"
        "s_sub_i32 s49, 268, s50\n"
        "s_lshl_b32 s40, s49, 23\n"
        "s_mul_i32 s51, s49, 0x800080\n"
        "s_sub_i32 s49, s50, 14\n"
        "s_mul_i32 s41, s49, 0x800080\n"
        "s_lshl_b32 s42, s48, 16\n"
        "v_and_b32 v2, s51, v16\n"
        "v_and_b32 v3, s51, v17\n"
        "v_mov_b32 v6, s40\n"
        "s_nop 0\n"
        "v_mfma_f32_16x16x16_bf16 v[18:21], v[2:3], v[30:31], 0\n"
        "v_mul_f32 v6, s42, v6\n"
        "v_and_b32 v24, s41, v16\n"
        "v_and_b32 v25, s41, v17\n"
        "v_mov_b32 v7, v6\n"
        "v_mov_b32 v8, v6\n"
        "v_mov_b32 v9, v6\n"
        "ds_write_b128 v11, v[6:9] offset:1024\n"
        "s_nop 0\n"
        "v_cvt_pkrtz_f16_f32 v2, v18, v19\n"
        "v_cvt_pkrtz_f16_f32 v3, v20, v21\n"
        "ds_write_b64 v4, v[2:3] offset:2168\n"
        "v_and_b32 v22, v12, v2\n"
        "v_and_b32 v23, v12, v3\n"
        "ds_write_b128 v11, v[22:25]\n"
        "ds_write_b32 v11, v5 offset:3328\n"
        "s_waitcnt lgkmcnt(0)\n"
        "s_waitcnt vmcnt(0)\n"
        "s_mov_b64 exec, 0\n"
        "s_branch STUB%=\n"
        "C2%=:\n"
        "ds_read_b128 v[32:35], v10 offset:13632\n"
        "ds_read_b128 v[38:41], v10 offset:12608\n"
        "ds_read_b128 v[42:45], v10 offset:11584\n"
        "s_mov_b32 m0, s39\n"
        "s_mov_b32 exec_hi, 0xffffff\n"
        "global_load_lds_dwordx4 v30, s[20:21] nt\n"
        "s_mov_b32 exec_hi, -1\n"
        "s_add_u32 m0, s36, 3584\n"
        "s_add_u32 s18, s18, 3200\n"
        "s_addc_u32 s19, s19, 0\n"
        "global_load_lds_dwordx4 v7, s[18:19] offset:512 nt\n"
        "s_waitcnt vmcnt(6)\n"
        "v_mov_b32_dpp v24, v20 row_newbcast:0 row_mask:0xf bank_mask:0xf\n"
        "v_mov_b32_dpp v25, v21 row_newbcast:0 row_mask:0xf bank_mask:0xf\n"
        "v_and_b32_dpp v46, v20, v15 row_newbcast:0 row_mask:0xf bank_mask:0xf\n"
        "v_and_b32_dpp v47, v20, v16 row_newbcast:0 row_mask:0xf bank_mask:0xf\n"
        "v_and_b32_dpp v48, v21, v15 row_newbcast:0 row_mask:0xf bank_mask:0xf\n"
        "v_and_b32_dpp v49, v21, v16 row_newbcast:0 row_mask:0xf bank_mask:0xf\n"
        "s_waitcnt lgkmcnt(2)\n"
        "v_mfma_f32_16x16x32_bf16 v[50:53], v[32:35], v[24:27], 0\n"
        "s_waitcnt lgkmcnt(0)\n"
        "v_dot2_f32_bf16 v54, v46, v40, v42\n"
        "v_dot2_f32_bf16 v55, v47, v40, v43\n"
        "v_dot2_f32_bf16 v56, v48, v41, v44\n"
        "v_dot2_f32_bf16 v57, v49, v41, v45\n"
        "s_waitcnt vmcnt(5)\n"
        "ds_read_b64 v[4:5], v2 offset:512\n"
        "ds_read_b64 v[58:59], v2\n"
        "v_mfma_f32_16x16x32_bf16 v[24:27], v[32:35], v[20:23], 0\n"
        "v_and_b32 v62, v15, v20\n"
        "v_and_b32 v63, v16, v20\n"
        "v_cvt_pk_bf16_f32 v30, v54, v55\n"
        "v_cvt_pk_bf16_f32 v31, v56, v57\n"
        "v_and_b32 v64, v15, v21\n"
        "v_and_b32 v65, v16, v21\n"
        "v_and_b32 v46, v28, v62\n"
        "v_and_b32 v47, v14, v64\n"
        "v_and_b32 v48, v28, v63\n"
        "v_and_b32 v49, v14, v65\n"
        "ds_write_b128 v18, v[46:49] offset:13312\n"
        "ds_write_b128 v9, v[24:27] offset:12800\n"
        "s_waitcnt lgkmcnt(3)\n"
        "v_mfma_f32_16x16x16_bf16 v[32:35], v[30:31], v[4:5], v[50:53]\n"
        "s_waitcnt lgkmcnt(2)\n"
        "v_dot2_f32_bf16 v20, v58, v62, 0\n"
        "v_dot2_f32_bf16 v21, v58, v63, 0\n"
        "v_dot2_f32_bf16 v22, v59, v64, 0\n"
        "v_dot2_f32_bf16 v23, v59, v65, 0\n"
        "ds_read_b64 v[14:15], v19 offset:13472\n"
        "s_nop 1\n"
        "v_exp_f32 v46, v32\n"
        "v_exp_f32 v47, v33\n"
        "v_exp_f32 v48, v34\n"
        "v_cvt_pk_bf16_f32 v60, v20, v21\n"
        "v_cvt_pk_bf16_f32 v61, v22, v23\n"
        "v_exp_f32 v49, v35\n"
        "v_cvt_pkrtz_f16_f32 v46, v46, v47\n"
        "v_cvt_pkrtz_f16_f32 v48, v48, v49\n"
        "v_mfma_f32_16x16x32_bf16 v[54:57], v[38:41], v[58:61], v[24:27]\n"
        "v_pk_add_f16 v46, v46, 1.0 op_sel_hi:[1,0]\n"
        "v_pk_add_f16 v48, v48, 1.0 op_sel_hi:[1,0]\n"
        "v_rcp_f16 v62, v46\n"
        "v_rcp_f16 v63, v48\n"
        "s_waitcnt lgkmcnt(0)\n"
        "v_mfma_f32_16x16x16_bf16 v[20:23], v[14:15], v[40:41], v[42:45]\n"
        "v_rcp_f16_sdwa v62, v46 dst_sel:WORD_1 dst_unused:UNUSED_PRESERVE src0_sel:WORD_1\n"
        "ds_read_b32 v3, v10 offset:3328\n"
        "ds_read_b128 v[24:27], v10 offset:1024\n"
        "ds_read_b128 v[68:71], v10\n"
        "ds_read_b64 v[66:67], v17 offset:120\n"
        "v_rcp_f16_sdwa v63, v48 dst_sel:WORD_1 dst_unused:UNUSED_PRESERVE src0_sel:WORD_1\n"
        "v_exp_f32 v32, v54\n"
        "v_exp_f32 v33, v55\n"
        "s_waitcnt vmcnt(4)\n"
        "ds_read_b64 v[4:5], v2 offset:1024\n"
        "ds_read_b64 v[14:15], v2 offset:1536\n"
        "global_load_lds_dwordx4 v12, s[18:19] offset:1536 nt\n"
        "v_exp_f32 v34, v56\n"
        "v_exp_f32 v35, v57\n"
        "v_cvt_pkrtz_f16_f32 v32, v32, v33\n"
        "v_cvt_pkrtz_f16_f32 v34, v34, v35\n"
        "v_cvt_pk_bf16_f32 v46, v20, v21\n"
        "v_cvt_pk_bf16_f32 v47, v22, v23\n"
        "v_pk_add_f16 v32, v32, 1.0 op_sel_hi:[1,0]\n"
        "s_waitcnt lgkmcnt(5)\n"
        "v_cmp_eq_u32 vcc, 0, v3\n"
        "v_pk_add_f16 v34, v34, 1.0 op_sel_hi:[1,0]\n"
        "v_rcp_f16 v64, v32\n"
        "s_waitcnt lgkmcnt(2)\n"
        "s_cbranch_vccnz SLOW%=\n"
        "BACK%=:\n"
        "s_waitcnt lgkmcnt(1)\n"
        "v_mfma_f32_16x16x16_bf16 v[54:57], v[30:31], v[4:5], v[50:53]\n"
        "v_rcp_f16 v65, v34\n"
        "v_rcp_f16_sdwa v64, v32 dst_sel:WORD_1 dst_unused:UNUSED_PRESERVE src0_sel:WORD_1\n"
        "s_waitcnt lgkmcnt(0)\n"
        "v_mfma_f32_16x16x16_bf16 v[20:23], v[30:31], v[14:15], v[50:53]\n"
        "v_rcp_f16_sdwa v65, v34 dst_sel:WORD_1 dst_unused:UNUSED_PRESERVE src0_sel:WORD_1\n"
        "s_setprio 2\n"
        "ds_read_b64 v[48:49], v19 offset:13632\n"
        "s_nop 0\n"
        "v_exp_f32 v58, v54\n"
        "v_exp_f32 v59, v55\n"
        "v_mfma_f32_16x16x32_f16 v[72:75], v[62:65], v[66:69], v[24:27]\n"
        "v_exp_f32 v60, v56\n"
        "v_exp_f32 v61, v57\n"
        "v_cvt_pkrtz_f16_f32 v58, v58, v59\n"
        "ds_read2_b64 v[30:33], v17 offset0:15 offset1:14\n"
        "ds_read_b128 v[50:53], v29 offset:12864\n"
        "v_cvt_pkrtz_f16_f32 v60, v60, v61\n"
        "v_pk_add_f16 v58, v58, 1.0 op_sel_hi:[1,0]\n"
        "v_pk_add_f16 v60, v60, 1.0 op_sel_hi:[1,0]\n"
        "v_rcp_f16 v62, v58\n"
        "v_rcp_f16 v63, v60\n"
        "s_waitcnt lgkmcnt(2)\n"
        "v_mfma_f32_16x16x16_bf16 v[54:57], v[48:49], v[40:41], v[42:45]\n"
        "v_rcp_f16_sdwa v62, v58 dst_sel:WORD_1 dst_unused:UNUSED_PRESERVE src0_sel:WORD_1\n"
        "v_rcp_f16_sdwa v63, v60 dst_sel:WORD_1 dst_unused:UNUSED_PRESERVE src0_sel:WORD_1\n"
        "v_exp_f32 v76, v20\n"
        "v_exp_f32 v77, v21\n"
        "s_waitcnt vmcnt(4)\n"
        "ds_read_b64 v[4:5], v2 offset:2048\n"
        "ds_read_b64 v[14:15], v2 offset:2560\n"
        "global_load_lds_dwordx4 v12, s[18:19] offset:2560 nt\n"
        "v_exp_f32 v78, v22\n"
        "v_exp_f32 v79, v23\n"
        "v_cvt_pkrtz_f16_f32 v76, v76, v77\n"
        "v_cvt_pkrtz_f16_f32 v78, v78, v79\n"
        "v_cvt_pk_bf16_f32 v34, v54, v55\n"
        "v_cvt_pk_bf16_f32 v35, v56, v57\n"
        "v_pk_add_f16 v76, v76, 1.0 op_sel_hi:[1,0]\n"
        "v_pk_add_f16 v78, v78, 1.0 op_sel_hi:[1,0]\n"
        "v_rcp_f16 v64, v76\n"
        "s_waitcnt lgkmcnt(1)\n"
        "v_mfma_f32_16x16x16_bf16 v[20:23], v[46:47], v[4:5], v[50:53]\n"
        "v_rcp_f16 v65, v78\n"
        "v_rcp_f16_sdwa v64, v76 dst_sel:WORD_1 dst_unused:UNUSED_PRESERVE src0_sel:WORD_1\n"
        "s_waitcnt lgkmcnt(0)\n"
        "v_mfma_f32_16x16x16_bf16 v[58:61], v[46:47], v[14:15], v[50:53]\n"
        "v_rcp_f16_sdwa v65, v78 dst_sel:WORD_1 dst_unused:UNUSED_PRESERVE src0_sel:WORD_1\n"
        "s_setprio 1\n"
        "ds_read_b64 v[48:49], v19 offset:13792\n"
        "s_nop 0\n"
        "v_exp_f32 v54, v20\n"
        "v_exp_f32 v55, v21\n"
        "v_mfma_f32_16x16x32_f16 v[80:83], v[62:65], v[30:33], v[24:27]\n"
        "v_exp_f32 v56, v22\n"
        "v_exp_f32 v57, v23\n"
        "v_cvt_pkrtz_f16_f32 v54, v54, v55\n"
        "ds_read2_b64 v[76:79], v17 offset0:11 offset1:10\n"
        "ds_read_b128 v[84:87], v29 offset:12928\n"
        "v_cvt_pkrtz_f16_f32 v56, v56, v57\n"
        "v_pk_add_f16 v54, v54, 1.0 op_sel_hi:[1,0]\n"
        "v_pk_add_f16 v56, v56, 1.0 op_sel_hi:[1,0]\n"
        "v_rcp_f16 v24, v54\n"
        "v_rcp_f16 v25, v56\n"
        "s_waitcnt lgkmcnt(2)\n"
        "v_mfma_f32_16x16x16_bf16 v[20:23], v[48:49], v[40:41], v[42:45]\n"
        "v_rcp_f16_sdwa v24, v54 dst_sel:WORD_1 dst_unused:UNUSED_PRESERVE src0_sel:WORD_1\n"
        "v_rcp_f16_sdwa v25, v56 dst_sel:WORD_1 dst_unused:UNUSED_PRESERVE src0_sel:WORD_1\n"
        "v_exp_f32 v30, v58\n"
        "v_exp_f32 v31, v59\n"
        "s_waitcnt vmcnt(4)\n"
        "ds_read_b64 v[4:5], v2 offset:3072\n"
        "ds_read_b64 v[14:15], v2 offset:3584\n"
        "s_add_u32 m0, s36, 6656\n"
        "s_add_u32 s18, s18, 3200\n"
        "s_addc_u32 s19, s19, 0\n"
        "global_load_lds_dwordx4 v7, s[18:19] offset:512 nt\n"
        "v_exp_f32 v32, v60\n"
        "v_exp_f32 v33, v61\n"
        "v_cvt_pkrtz_f16_f32 v30, v30, v31\n"
        "v_cvt_pkrtz_f16_f32 v32, v32, v33\n"
        "v_cvt_pk_bf16_f32 v48, v20, v21\n"
        "v_cvt_pk_bf16_f32 v49, v22, v23\n"
        "v_pk_add_f16 v30, v30, 1.0 op_sel_hi:[1,0]\n"
        "v_pk_add_f16 v32, v32, 1.0 op_sel_hi:[1,0]\n"
        "v_rcp_f16 v26, v30\n"
        "s_waitcnt lgkmcnt(1)\n"
        "v_mfma_f32_16x16x16_bf16 v[54:57], v[46:47], v[4:5], v[50:53]\n"
        "v_rcp_f16 v27, v32\n"
        "v_rcp_f16_sdwa v26, v30 dst_sel:WORD_1 dst_unused:UNUSED_PRESERVE src0_sel:WORD_1\n"
        "s_waitcnt lgkmcnt(0)\n"
        "v_mfma_f32_16x16x16_bf16 v[58:61], v[34:35], v[14:15], v[84:87]\n"
        "v_rcp_f16_sdwa v27, v32 dst_sel:WORD_1 dst_unused:UNUSED_PRESERVE src0_sel:WORD_1\n"
        "s_setprio 0\n"
        "ds_read_b64 v[20:21], v19 offset:13952\n"
        "s_waitcnt vmcnt(4)\n"
        "ds_read2_b32 v[4:5], v1 offset0:0 offset1:1\n"
        "ds_read2_b32 v[22:23], v13 offset0:0 offset1:1\n"
        "v_exp_f32 v50, v54\n"
        "v_exp_f32 v51, v55\n"
        "v_mfma_f32_16x16x32_f16 v[80:83], v[24:27], v[76:79], v[80:83]\n"
        "v_exp_f32 v52, v56\n"
        "v_exp_f32 v53, v57\n"
        "v_cvt_pkrtz_f16_f32 v50, v50, v51\n"
        "ds_read2_b64 v[30:33], v17 offset0:11 offset1:7\n"
        "v_cvt_pkrtz_f16_f32 v52, v52, v53\n"
        "v_pk_add_f16 v50, v50, 1.0 op_sel_hi:[1,0]\n"
        "v_pk_add_f16 v52, v52, 1.0 op_sel_hi:[1,0]\n"
        "v_rcp_f16 v24, v50\n"
        "s_waitcnt lgkmcnt(1)\n"
        "v_and_b32 v22, v36, v22\n"
        "v_and_b32 v23, v37, v23\n"
        "v_rcp_f16 v25, v52\n"
        "v_mfma_f32_16x16x16_bf16 v[54:57], v[20:21], v[40:41], v[42:45]\n"
        "v_rcp_f16_sdwa v24, v50 dst_sel:WORD_1 dst_unused:UNUSED_PRESERVE src0_sel:WORD_1\n"
        "v_rcp_f16_sdwa v25, v52 dst_sel:WORD_1 dst_unused:UNUSED_PRESERVE src0_sel:WORD_1\n"
        "v_mfma_f32_16x16x16_bf16 v[62:65], v[70:71], v[22:23], 0\n"
        "v_exp_f32 v76, v58\n"
        "v_exp_f32 v77, v59\n"
        "s_waitcnt vmcnt(3)\n"
        "ds_read_b64 v[14:15], v2 offset:4096\n"
        "ds_read_b64 v[20:21], v2 offset:4608\n"
        "global_load_lds_dwordx4 v12, s[18:19] offset:1536 nt\n"
        "v_mfma_f32_16x16x16_bf16 v[50:53], v[70:71], v[4:5], 0\n"
        "v_exp_f32 v78, v60\n"
        "v_exp_f32 v79, v61\n"
        "v_cvt_pkrtz_f16_f32 v76, v76, v77\n"
        "v_cvt_pkrtz_f16_f32 v78, v78, v79\n"
        "v_cvt_pk_bf16_f32 v22, v54, v55\n"
        "v_cvt_pk_bf16_f32 v23, v56, v57\n"
        "v_pk_add_f16 v76, v76, 1.0 op_sel_hi:[1,0]\n"
        "v_pk_add_f16 v78, v78, 1.0 op_sel_hi:[1,0]\n"
        "v_rcp_f16 v26, v76\n"
        "s_waitcnt lgkmcnt(1)\n"
        "v_mfma_f32_16x16x16_bf16 v[58:61], v[34:35], v[14:15], v[84:87]\n"
        "v_rcp_f16 v27, v78\n"
        "v_rcp_f16_sdwa v26, v76 dst_sel:WORD_1 dst_unused:UNUSED_PRESERVE src0_sel:WORD_1\n"
        "s_waitcnt lgkmcnt(0)\n"
        "v_mfma_f32_16x16x16_bf16 v[66:69], v[34:35], v[20:21], v[84:87]\n"
        "v_rcp_f16_sdwa v27, v78 dst_sel:WORD_1 dst_unused:UNUSED_PRESERVE src0_sel:WORD_1\n"
        "s_setprio 3\n"
        "ds_read_b64 v[4:5], v19 offset:14112\n"
        "ds_read_b64_tr_b16 v[54:55], v11 offset:1024\n"
        "ds_read_b64_tr_b16 v[56:57], v11 offset:512\n"
        "ds_read_b64_tr_b16 v[88:89], v11 offset:1536\n"
        "ds_read_b64_tr_b16 v[90:91], v11\n"
        "v_exp_f32 v34, v58\n"
        "v_exp_f32 v35, v59\n"
        "v_mfma_f32_16x16x32_f16 v[72:75], v[24:27], v[30:33], v[72:75]\n"
        "v_exp_f32 v36, v60\n"
        "v_exp_f32 v37, v61\n"
        "v_cvt_pkrtz_f16_f32 v34, v34, v35\n"
        "ds_read2_b64 v[76:79], v17 offset0:7 offset1:6\n"
        "ds_read_b128 v[84:87], v29 offset:12992\n"
        "v_cvt_pkrtz_f16_f32 v36, v36, v37\n"
        "v_pk_add_f16 v34, v34, 1.0 op_sel_hi:[1,0]\n"
        "v_pk_add_f16 v36, v36, 1.0 op_sel_hi:[1,0]\n"
        "v_rcp_f16 v24, v34\n"
        "v_rcp_f16 v25, v36\n"
        "s_waitcnt lgkmcnt(6)\n"
        "v_mfma_f32_16x16x16_bf16 v[30:33], v[4:5], v[40:41], v[42:45]\n"
        "v_pk_mul_f32 v[58:59], v[72:73], v[62:63]\n"
        "v_pk_mul_f32 v[60:61], v[74:75], v[64:65]\n"
        "v_cvt_pk_bf16_f32 v94, v58, v59\n"
        "v_cvt_pk_bf16_f32 v95, v60, v61\n"
        "v_rcp_f16_sdwa v24, v34 dst_sel:WORD_1 dst_unused:UNUSED_PRESERVE src0_sel:WORD_1\n"
        "v_rcp_f16_sdwa v25, v36 dst_sel:WORD_1 dst_unused:UNUSED_PRESERVE src0_sel:WORD_1\n"
        "v_exp_f32 v96, v66\n"
        "v_exp_f32 v97, v67\n"
        "s_waitcnt vmcnt(3)\n"
        "ds_read_b64 v[4:5], v2 offset:5120\n"
        "ds_read_b64 v[14:15], v2 offset:5632\n"
        "global_load_lds_dwordx4 v12, s[18:19] offset:2560 nt\n"
        "v_exp_f32 v98, v68\n"
        "v_exp_f32 v99, v69\n"
        "ds_read_b64_tr_b16 v[34:35], v11 offset:2048\n"
        "ds_read_b64_tr_b16 v[36:37], v11 offset:3072\n"
        "ds_read_b64_tr_b16 v[58:59], v11 offset:2560\n"
        "ds_read_b64_tr_b16 v[60:61], v11\n"
        "v_cvt_pkrtz_f16_f32 v96, v96, v97\n"
        "v_cvt_pkrtz_f16_f32 v98, v98, v99\n"
        "v_cvt_pk_bf16_f32 v20, v30, v31\n"
        "v_cvt_pk_bf16_f32 v21, v32, v33\n"
        "v_pk_add_f16 v96, v96, 1.0 op_sel_hi:[1,0]\n"
        "v_pk_add_f16 v98, v98, 1.0 op_sel_hi:[1,0]\n"
        "v_rcp_f16 v26, v96\n"
        "s_waitcnt lgkmcnt(5)\n"
        "v_mfma_f32_16x16x16_bf16 v[66:69], v[48:49], v[4:5], v[84:87]\n"
        "v_rcp_f16 v27, v98\n"
        "v_rcp_f16_sdwa v26, v96 dst_sel:WORD_1 dst_unused:UNUSED_PRESERVE src0_sel:WORD_1\n"
        "s_waitcnt lgkmcnt(4)\n"
        "v_mfma_f32_16x16x16_bf16 v[100:103], v[48:49], v[14:15], v[84:87]\n"
        "v_rcp_f16_sdwa v27, v98 dst_sel:WORD_1 dst_unused:UNUSED_PRESERVE src0_sel:WORD_1\n"
        "s_setprio 2\n"
        "ds_read_b64 v[30:31], v19 offset:14272\n"
        "ds_read_b64_tr_b16 v[104:105], v11 offset:4096\n"
        "ds_read_b64_tr_b16 v[106:107], v11 offset:3584\n"
        "ds_read_b64_tr_b16 v[108:109], v11 offset:4608\n"
        "ds_read_b64_tr_b16 v[110:111], v11\n"
        "v_exp_f32 v112, v66\n"
        "v_exp_f32 v113, v67\n"
        "v_mfma_f32_16x16x32_f16 v[80:83], v[24:27], v[76:79], v[80:83]\n"
        "v_exp_f32 v114, v68\n"
        "v_exp_f32 v115, v69\n"
        "v_cvt_pkrtz_f16_f32 v112, v112, v113\n"
        "ds_read2_b64 v[96:99], v17 offset0:3 offset1:2\n"
        "ds_read_b128 v[116:119], v29 offset:13056\n"
        "v_cvt_pkrtz_f16_f32 v114, v114, v115\n"
        "v_pk_add_f16 v112, v112, 1.0 op_sel_hi:[1,0]\n"
        "v_pk_add_f16 v114, v114, 1.0 op_sel_hi:[1,0]\n"
        "v_rcp_f16 v24, v112\n"
        "v_rcp_f16 v25, v114\n"
        "s_waitcnt lgkmcnt(6)\n"
        "v_mfma_f32_16x16x16_bf16 v[66:69], v[30:31], v[40:41], v[42:45]\n"
        "v_pk_mul_f32 v[76:77], v[80:81], v[50:51]\n"
        "v_pk_mul_f32 v[78:79], v[82:83], v[52:53]\n"
        "v_cvt_pk_bf16_f32 v92, v76, v77\n"
        "v_cvt_pk_bf16_f32 v93, v78, v79\n"
        "v_rcp_f16_sdwa v24, v112 dst_sel:WORD_1 dst_unused:UNUSED_PRESERVE src0_sel:WORD_1\n"
        "v_rcp_f16_sdwa v25, v114 dst_sel:WORD_1 dst_unused:UNUSED_PRESERVE src0_sel:WORD_1\n"
        "v_exp_f32 v120, v100\n"
        "v_exp_f32 v121, v101\n"
        "s_waitcnt vmcnt(3)\n"
        "ds_read_b64 v[4:5], v2 offset:6144\n"
        "ds_read_b64 v[14:15], v2 offset:6656\n"
        "s_add_u32 m0, s36, 9728\n"
        "s_add_u32 s18, s18, 3200\n"
        "s_addc_u32 s19, s19, 0\n"
        "global_load_lds_dwordx4 v7, s[18:19] offset:512 nt\n"
        "v_mfma_f32_16x16x32_bf16 v[30:33], v[92:95], v[54:57], 0\n"
        "v_mfma_f32_16x16x32_bf16 v[76:79], v[92:95], v[88:91], 0\n"
        "v_exp_f32 v122, v102\n"
        "v_exp_f32 v123, v103\n"
        "v_mfma_f32_16x16x32_bf16 v[112:115], v[92:95], v[34:37], 0\n"
        "v_mfma_f32_16x16x32_bf16 v[124:127], v[92:95], v[58:61], 0\n"
        "v_cvt_pkrtz_f16_f32 v120, v120, v121\n"
        "v_cvt_pkrtz_f16_f32 v122, v122, v123\n"
        "v_cvt_pk_bf16_f32 v6, v66, v67\n"
        "v_cvt_pk_bf16_f32 v7, v68, v69\n"
        "v_pk_add_f16 v120, v120, 1.0 op_sel_hi:[1,0]\n"
        "s_waitcnt lgkmcnt(6)\n"
        "v_mfma_f32_16x16x32_bf16 v[54:57], v[92:95], v[104:107], 0\n"
        "s_waitcnt lgkmcnt(4)\n"
        "v_mfma_f32_16x16x32_bf16 v[34:37], v[92:95], v[108:111], 0\n"
        "v_pk_add_f16 v122, v122, 1.0 op_sel_hi:[1,0]\n"
        "v_rcp_f16 v26, v120\n"
        "s_waitcnt lgkmcnt(1)\n"
        "v_mfma_f32_16x16x16_bf16 v[58:61], v[48:49], v[4:5], v[84:87]\n"
        "v_rcp_f16 v27, v122\n"
        "v_add_f32_dpp v46, v30, v77 quad_perm:[0,1,2,3] row_mask:0x1 bank_mask:0xf\n"
        "v_rcp_f16_sdwa v26, v120 dst_sel:WORD_1 dst_unused:UNUSED_PRESERVE src0_sel:WORD_1\n"
        "s_waitcnt lgkmcnt(0)\n"
        "v_mfma_f32_16x16x16_bf16 v[66:69], v[22:23], v[14:15], v[116:119]\n"
        "v_rcp_f16_sdwa v27, v122 dst_sel:WORD_1 dst_unused:UNUSED_PRESERVE src0_sel:WORD_1\n"
        "v_add_f32_dpp v46, v112, v125 quad_perm:[0,1,2,3] row_mask:0x2 bank_mask:0xf\n"
        "s_setprio 1\n"
        "ds_read_b64 v[4:5], v19 offset:14432\n"
        "v_add_f32_dpp v46, v54, v35 quad_perm:[0,1,2,3] row_mask:0x4 bank_mask:0xf\n"
        "v_exp_f32 v30, v58\n"
        "v_exp_f32 v31, v59\n"
        "v_mfma_f32_16x16x32_f16 v[80:83], v[24:27], v[96:99], v[80:83]\n"
        "v_exp_f32 v32, v60\n"
        "v_exp_f32 v33, v61\n"
        "v_cvt_pkrtz_f16_f32 v30, v30, v31\n"
        "ds_read2_b64 v[76:79], v17 offset0:3 offset1:13\n"
        "v_cvt_pkrtz_f16_f32 v32, v32, v33\n"
        "v_pk_add_f16 v30, v30, 1.0 op_sel_hi:[1,0]\n"
        "v_pk_add_f16 v32, v32, 1.0 op_sel_hi:[1,0]\n"
        "v_rcp_f16 v24, v30\n"
        "v_rcp_f16 v25, v32\n"
        "s_waitcnt lgkmcnt(1)\n"
        "v_mfma_f32_16x16x16_bf16 v[34:37], v[4:5], v[40:41], v[42:45]\n"
        "v_rcp_f16_sdwa v24, v30 dst_sel:WORD_1 dst_unused:UNUSED_PRESERVE src0_sel:WORD_1\n"
        "v_rcp_f16_sdwa v25, v32 dst_sel:WORD_1 dst_unused:UNUSED_PRESERVE src0_sel:WORD_1\n"
        "v_exp_f32 v54, v66\n"
        "v_exp_f32 v55, v67\n"
        "s_waitcnt vmcnt(3)\n"
        "ds_read_b64 v[14:15], v2 offset:7168\n"
        "ds_read_b64 v[18:19], v2 offset:7680\n"
        "global_load_lds_dwordx4 v12, s[18:19] offset:1536 nt\n"
        "v_exp_f32 v56, v68\n"
        "v_exp_f32 v57, v69\n"
        "v_cvt_pkrtz_f16_f32 v54, v54, v55\n"
        "v_cvt_pkrtz_f16_f32 v56, v56, v57\n"
        "v_cvt_pk_bf16_f32 v4, v34, v35\n"
        "v_cvt_pk_bf16_f32 v5, v36, v37\n"
        "v_pk_add_f16 v54, v54, 1.0 op_sel_hi:[1,0]\n"
        "v_pk_add_f16 v56, v56, 1.0 op_sel_hi:[1,0]\n"
        "v_rcp_f16 v26, v54\n"
        "s_waitcnt lgkmcnt(1)\n"
        "v_mfma_f32_16x16x16_bf16 v[30:33], v[22:23], v[14:15], v[116:119]\n"
        "v_rcp_f16 v27, v56\n"
        "v_rcp_f16_sdwa v26, v54 dst_sel:WORD_1 dst_unused:UNUSED_PRESERVE src0_sel:WORD_1\n"
        "s_waitcnt lgkmcnt(0)\n"
        "v_mfma_f32_16x16x16_bf16 v[38:41], v[22:23], v[18:19], v[116:119]\n"
        "v_rcp_f16_sdwa v27, v56 dst_sel:WORD_1 dst_unused:UNUSED_PRESERVE src0_sel:WORD_1\n"
        "s_setprio 0\n"
        "ds_read_b64_tr_b16 v[34:35], v11 offset:5120\n"
        "ds_read_b64_tr_b16 v[36:37], v11 offset:6144\n"
        "ds_read_b64_tr_b16 v[42:43], v11 offset:5632\n"
        "ds_read_b64_tr_b16 v[44:45], v11\n"
        "v_exp_f32 v58, v30\n"
        "v_exp_f32 v59, v31\n"
        "v_mfma_f32_16x16x32_f16 v[72:75], v[24:27], v[76:79], v[72:75]\n"
        "v_exp_f32 v60, v32\n"
        "v_exp_f32 v61, v33\n"
        "v_cvt_pkrtz_f16_f32 v58, v58, v59\n"
        "ds_read2_b64 v[54:57], v17 offset0:13 offset1:12\n"
        "ds_read_b128 v[66:69], v29 offset:13120\n"
        "v_cvt_pkrtz_f16_f32 v60, v60, v61\n"
        "v_pk_add_f16 v58, v58, 1.0 op_sel_hi:[1,0]\n"
        "STUB%=:\n"
        "s_cbranch_execz FIN%=\n"
        "v_pk_add_f16 v60, v60, 1.0 op_sel_hi:[1,0]\n"
        "v_rcp_f16 v22, v58\n"
        "v_rcp_f16 v23, v60\n"
        "v_pk_mul_f32 v[30:31], v[72:73], v[62:63]\n"
        "v_pk_mul_f32 v[32:33], v[74:75], v[64:65]\n"
        "v_cvt_pk_bf16_f32 v94, v30, v31\n"
        "v_cvt_pk_bf16_f32 v95, v32, v33\n"
        "v_rcp_f16_sdwa v22, v58 dst_sel:WORD_1 dst_unused:UNUSED_PRESERVE src0_sel:WORD_1\n"
        "v_rcp_f16_sdwa v23, v60 dst_sel:WORD_1 dst_unused:UNUSED_PRESERVE src0_sel:WORD_1\n"
        "v_exp_f32 v76, v38\n"
        "v_exp_f32 v77, v39\n"
        "s_waitcnt vmcnt(3)\n"
        "ds_read_b64 v[14:15], v2 offset:8192\n"
        "ds_read_b64 v[18:19], v2 offset:8704\n"
        "s_mov_b32 exec_hi, 0\n"
        "global_load_lds_dwordx4 v12, s[18:19] offset:2560 nt\n"
        "s_mov_b32 exec_hi, -1\n"
        "v_exp_f32 v78, v40\n"
        "v_exp_f32 v79, v41\n"
        "v_cvt_pkrtz_f16_f32 v76, v76, v77\n"
        "v_cvt_pkrtz_f16_f32 v78, v78, v79\n"
        "v_pk_add_f16 v76, v76, 1.0 op_sel_hi:[1,0]\n"
        "v_pk_add_f16 v78, v78, 1.0 op_sel_hi:[1,0]\n"
        "v_rcp_f16 v24, v76\n"
        "s_waitcnt lgkmcnt(1)\n"
        "v_mfma_f32_16x16x16_bf16 v[30:33], v[20:21], v[14:15], v[66:69]\n"
        "v_rcp_f16 v25, v78\n"
        "v_rcp_f16_sdwa v24, v76 dst_sel:WORD_1 dst_unused:UNUSED_PRESERVE src0_sel:WORD_1\n"
        "s_waitcnt lgkmcnt(0)\n"
        "v_mfma_f32_16x16x16_bf16 v[38:41], v[20:21], v[18:19], v[66:69]\n"
        "v_rcp_f16_sdwa v25, v78 dst_sel:WORD_1 dst_unused:UNUSED_PRESERVE src0_sel:WORD_1\n"
        "s_setprio 3\n"
        "s_nop 1\n"
        "v_exp_f32 v58, v30\n"
        "v_exp_f32 v59, v31\n"
        "v_mfma_f32_16x16x32_f16 v[80:83], v[22:25], v[54:57], v[80:83]\n"
        "v_exp_f32 v60, v32\n"
        "v_exp_f32 v61, v33\n"
        "v_cvt_pkrtz_f16_f32 v58, v58, v59\n"
        "ds_read2_b64 v[12:15], v17 offset0:9 offset1:8\n"
        "ds_read_b128 v[76:79], v29 offset:13184\n"
        "v_cvt_pkrtz_f16_f32 v60, v60, v61\n"
        "v_pk_add_f16 v58, v58, 1.0 op_sel_hi:[1,0]\n"
        "v_pk_add_f16 v60, v60, 1.0 op_sel_hi:[1,0]\n"
        "v_rcp_f16 v22, v58\n"
        "v_rcp_f16 v23, v60\n"
        "v_rcp_f16_sdwa v22, v58 dst_sel:WORD_1 dst_unused:UNUSED_PRESERVE src0_sel:WORD_1\n"
        "v_rcp_f16_sdwa v23, v60 dst_sel:WORD_1 dst_unused:UNUSED_PRESERVE src0_sel:WORD_1\n"
        "v_exp_f32 v30, v38\n"
        "v_exp_f32 v31, v39\n"
        "s_waitcnt vmcnt(3)\n"
        "ds_read_b64 v[18:19], v2 offset:9216\n"
        "ds_read_b64 v[26:27], v2 offset:9728\n"
        "v_exp_f32 v32, v40\n"
        "v_exp_f32 v33, v41\n"
        "v_cvt_pkrtz_f16_f32 v30, v30, v31\n"
        "v_cvt_pkrtz_f16_f32 v32, v32, v33\n"
        "v_pk_add_f16 v30, v30, 1.0 op_sel_hi:[1,0]\n"
        "v_pk_add_f16 v32, v32, 1.0 op_sel_hi:[1,0]\n"
        "v_rcp_f16 v24, v30\n"
        "s_waitcnt lgkmcnt(1)\n"
        "v_mfma_f32_16x16x16_bf16 v[54:57], v[20:21], v[18:19], v[66:69]\n"
        "v_rcp_f16 v25, v32\n"
        "v_rcp_f16_sdwa v24, v30 dst_sel:WORD_1 dst_unused:UNUSED_PRESERVE src0_sel:WORD_1\n"
        "s_waitcnt lgkmcnt(0)\n"
        "v_mfma_f32_16x16x16_bf16 v[38:41], v[6:7], v[26:27], v[76:79]\n"
        "v_rcp_f16_sdwa v25, v32 dst_sel:WORD_1 dst_unused:UNUSED_PRESERVE src0_sel:WORD_1\n"
        "s_setprio 2\n"
        "ds_read_b64_tr_b16 v[58:59], v11 offset:7168\n"
        "ds_read_b64_tr_b16 v[60:61], v11 offset:6656\n"
        "ds_read_b64_tr_b16 v[18:19], v11 offset:7680\n"
        "ds_read_b64_tr_b16 v[20:21], v11\n"
        "v_exp_f32 v66, v54\n"
        "v_exp_f32 v67, v55\n"
        "v_mfma_f32_16x16x32_f16 v[80:83], v[22:25], v[12:15], v[80:83]\n"
        "v_exp_f32 v68, v56\n"
        "v_exp_f32 v69, v57\n"
        "v_cvt_pkrtz_f16_f32 v66, v66, v67\n"
        "ds_read2_b64 v[30:33], v17 offset0:9 offset1:5\n"
        "v_cvt_pkrtz_f16_f32 v68, v68, v69\n"
        "v_pk_add_f16 v66, v66, 1.0 op_sel_hi:[1,0]\n"
        "v_pk_add_f16 v68, v68, 1.0 op_sel_hi:[1,0]\n"
        "v_rcp_f16 v12, v66\n"
        "v_rcp_f16 v13, v68\n"
        "v_pk_mul_f32 v[22:23], v[80:81], v[50:51]\n"
        "v_pk_mul_f32 v[24:25], v[82:83], v[52:53]\n"
        "v_cvt_pk_bf16_f32 v92, v22, v23\n"
        "v_cvt_pk_bf16_f32 v93, v24, v25\n"
        "v_rcp_f16_sdwa v12, v66 dst_sel:WORD_1 dst_unused:UNUSED_PRESERVE src0_sel:WORD_1\n"
        "v_rcp_f16_sdwa v13, v68 dst_sel:WORD_1 dst_unused:UNUSED_PRESERVE src0_sel:WORD_1\n"
        "v_exp_f32 v54, v38\n"
        "v_exp_f32 v55, v39\n"
        "s_waitcnt vmcnt(2)\n"
        "ds_read_b64 v[26:27], v2 offset:10240\n"
        "ds_read_b64 v[48:49], v2 offset:10752\n"
        "v_mfma_f32_16x16x32_bf16 v[22:25], v[92:95], v[34:37], 0\n"
        "v_mfma_f32_16x16x32_bf16 v[84:87], v[92:95], v[42:45], 0\n"
        "v_exp_f32 v56, v40\n"
        "v_exp_f32 v57, v41\n"
        "s_waitcnt lgkmcnt(5)\n"
        "v_mfma_f32_16x16x32_bf16 v[66:69], v[92:95], v[58:61], 0\n"
        "s_waitcnt lgkmcnt(3)\n"
        "v_mfma_f32_16x16x32_bf16 v[88:91], v[92:95], v[18:21], 0\n"
        "v_cvt_pkrtz_f16_f32 v54, v54, v55\n"
        "v_cvt_pkrtz_f16_f32 v56, v56, v57\n"
        "v_pk_add_f16 v54, v54, 1.0 op_sel_hi:[1,0]\n"
        "v_pk_add_f16 v56, v56, 1.0 op_sel_hi:[1,0]\n"
        "v_rcp_f16 v14, v54\n"
        "s_waitcnt lgkmcnt(1)\n"
        "v_mfma_f32_16x16x16_bf16 v[34:37], v[6:7], v[26:27], v[76:79]\n"
        "v_rcp_f16 v15, v56\n"
        "v_add_f32_dpp v46, v22, v85 quad_perm:[0,1,2,3] row_mask:0x8 bank_mask:0xf\n"
        "v_rcp_f16_sdwa v14, v54 dst_sel:WORD_1 dst_unused:UNUSED_PRESERVE src0_sel:WORD_1\n"
        "s_waitcnt lgkmcnt(0)\n"
        "v_mfma_f32_16x16x16_bf16 v[18:21], v[6:7], v[48:49], v[76:79]\n"
        "v_rcp_f16_sdwa v15, v56 dst_sel:WORD_1 dst_unused:UNUSED_PRESERVE src0_sel:WORD_1\n"
        "v_add_f32_dpp v47, v68, v91 quad_perm:[0,1,2,3] row_mask:0x1 bank_mask:0xf\n"
        "s_setprio 1\n"
        "ds_read_b64_tr_b16 v[38:39], v11 offset:8192\n"
        "ds_read_b64_tr_b16 v[40:41], v11 offset:9216\n"
        "ds_read_b64_tr_b16 v[22:23], v11 offset:8704\n"
        "ds_read_b64_tr_b16 v[24:25], v11\n"
        "v_exp_f32 v42, v34\n"
        "v_exp_f32 v43, v35\n"
        "v_mfma_f32_16x16x32_f16 v[72:75], v[12:15], v[30:33], v[72:75]\n"
        "v_exp_f32 v44, v36\n"
        "v_exp_f32 v45, v37\n"
        "v_cvt_pkrtz_f16_f32 v42, v42, v43\n"
        "ds_read2_b64 v[54:57], v17 offset0:5 offset1:4\n"
        "ds_read_b128 v[58:61], v29 offset:13248\n"
        "v_cvt_pkrtz_f16_f32 v44, v44, v45\n"
        "v_pk_add_f16 v42, v42, 1.0 op_sel_hi:[1,0]\n"
        "v_pk_add_f16 v44, v44, 1.0 op_sel_hi:[1,0]\n"
        "v_rcp_f16 v12, v42\n"
        "v_rcp_f16 v13, v44\n"
        "v_pk_mul_f32 v[30:31], v[72:73], v[62:63]\n"
        "v_pk_mul_f32 v[32:33], v[74:75], v[64:65]\n"
        "v_cvt_pk_bf16_f32 v94, v30, v31\n"
        "v_cvt_pk_bf16_f32 v95, v32, v33\n"
        "v_rcp_f16_sdwa v12, v42 dst_sel:WORD_1 dst_unused:UNUSED_PRESERVE src0_sel:WORD_1\n"
        "v_rcp_f16_sdwa v13, v44 dst_sel:WORD_1 dst_unused:UNUSED_PRESERVE src0_sel:WORD_1\n"
        "v_exp_f32 v26, v18\n"
        "v_exp_f32 v27, v19\n"
        "s_waitcnt vmcnt(1)\n"
        "ds_read_b64 v[6:7], v2 offset:11264\n"
        "ds_read_b64 v[34:35], v2 offset:11776\n"
        "s_waitcnt lgkmcnt(6)\n"
        "v_mfma_f32_16x16x32_bf16 v[30:33], v[92:95], v[38:41], 0\n"
        "s_waitcnt lgkmcnt(4)\n"
        "v_mfma_f32_16x16x32_bf16 v[66:69], v[92:95], v[22:25], 0\n"
        "v_exp_f32 v28, v20\n"
        "v_exp_f32 v29, v21\n"
        "ds_read_b64_tr_b16 v[42:43], v11 offset:10240\n"
        "ds_read_b64_tr_b16 v[44:45], v11 offset:9728\n"
        "ds_read_b64_tr_b16 v[76:77], v11 offset:10752\n"
        "ds_read_b64_tr_b16 v[78:79], v11\n"
        "v_cvt_pkrtz_f16_f32 v26, v26, v27\n"
        "v_cvt_pkrtz_f16_f32 v28, v28, v29\n"
        "v_pk_add_f16 v26, v26, 1.0 op_sel_hi:[1,0]\n"
        "v_pk_add_f16 v28, v28, 1.0 op_sel_hi:[1,0]\n"
        "v_rcp_f16 v14, v26\n"
        "s_waitcnt lgkmcnt(5)\n"
        "v_mfma_f32_16x16x16_bf16 v[18:21], v[4:5], v[6:7], v[58:61]\n"
        "v_rcp_f16 v15, v28\n"
        "v_add_f32_dpp v47, v32, v69 quad_perm:[0,1,2,3] row_mask:0x2 bank_mask:0xf\n"
        "v_rcp_f16_sdwa v14, v26 dst_sel:WORD_1 dst_unused:UNUSED_PRESERVE src0_sel:WORD_1\n"
        "s_waitcnt lgkmcnt(4)\n"
        "v_mfma_f32_16x16x16_bf16 v[22:25], v[4:5], v[34:35], v[58:61]\n"
        "v_rcp_f16_sdwa v15, v28 dst_sel:WORD_1 dst_unused:UNUSED_PRESERVE src0_sel:WORD_1\n"
        "s_setprio 0\n"
        "s_nop 0\n"
        "v_exp_f32 v36, v18\n"
        "v_exp_f32 v37, v19\n"
        "v_mfma_f32_16x16x32_f16 v[80:83], v[12:15], v[54:57], v[80:83]\n"
        "v_exp_f32 v38, v20\n"
        "v_exp_f32 v39, v21\n"
        "v_cvt_pkrtz_f16_f32 v36, v36, v37\n"
        "ds_read2_b64 v[26:29], v17 offset0:1 offset1:0\n"
        "ds_read_b64 v[6:7], v17 offset:8\n"
        "v_cvt_pkrtz_f16_f32 v38, v38, v39\n"
        "v_pk_add_f16 v36, v36, 1.0 op_sel_hi:[1,0]\n"
        "v_pk_add_f16 v38, v38, 1.0 op_sel_hi:[1,0]\n"
        "v_rcp_f16 v12, v36\n"
        "v_rcp_f16 v13, v38\n"
        "v_pk_mul_f32 v[18:19], v[80:81], v[50:51]\n"
        "v_pk_mul_f32 v[20:21], v[82:83], v[52:53]\n"
        "v_cvt_pk_bf16_f32 v92, v18, v19\n"
        "v_cvt_pk_bf16_f32 v93, v20, v21\n"
        "v_rcp_f16_sdwa v12, v36 dst_sel:WORD_1 dst_unused:UNUSED_PRESERVE src0_sel:WORD_1\n"
        "v_rcp_f16_sdwa v13, v38 dst_sel:WORD_1 dst_unused:UNUSED_PRESERVE src0_sel:WORD_1\n"
        "v_exp_f32 v30, v22\n"
        "v_exp_f32 v31, v23\n"
        "s_waitcnt vmcnt(0)\n"
        "ds_read_b64 v[16:17], v2 offset:12288\n"
        "ds_read_b64_tr_b16 v[56:57], v11 offset:12288\n"
        "s_waitcnt lgkmcnt(6)\n"
        "v_mfma_f32_16x16x32_bf16 v[18:21], v[92:95], v[42:45], 0\n"
        "s_waitcnt lgkmcnt(4)\n"
        "v_mfma_f32_16x16x32_bf16 v[66:69], v[92:95], v[76:79], 0\n"
        "v_exp_f32 v32, v24\n"
        "v_exp_f32 v33, v25\n"
        "ds_read_b64_tr_b16 v[54:55], v11 offset:11264\n"
        "ds_read_b64_tr_b16 v[34:35], v11 offset:11776\n"
        "ds_read_b64_tr_b16 v[36:37], v11\n"
        "v_cvt_pkrtz_f16_f32 v30, v30, v31\n"
        "v_cvt_pkrtz_f16_f32 v32, v32, v33\n"
        "v_pk_add_f16 v30, v30, 1.0 op_sel_hi:[1,0]\n"
        "v_pk_add_f16 v32, v32, 1.0 op_sel_hi:[1,0]\n"
        "v_rcp_f16 v14, v30\n"
        "s_waitcnt lgkmcnt(4)\n"
        "v_mfma_f32_16x16x16_bf16 v[22:25], v[4:5], v[16:17], v[58:61]\n"
        "v_rcp_f16 v15, v32\n"
        "v_add_f32_dpp v47, v20, v69 quad_perm:[0,1,2,3] row_mask:0x4 bank_mask:0xf\n"
        "v_rcp_f16_sdwa v14, v30 dst_sel:WORD_1 dst_unused:UNUSED_PRESERVE src0_sel:WORD_1\n"
        "v_rcp_f16_sdwa v15, v32 dst_sel:WORD_1 dst_unused:UNUSED_PRESERVE src0_sel:WORD_1\n"
        "s_setprio 3\n"
        "s_nop 2\n"
        "v_exp_f32 v38, v22\n"
        "v_exp_f32 v39, v23\n"
        "v_mfma_f32_16x16x32_f16 v[80:83], v[12:15], v[26:29], v[80:83]\n"
        "v_exp_f32 v40, v24\n"
        "v_exp_f32 v41, v25\n"
        "v_cvt_pkrtz_f16_f32 v38, v38, v39\n"
        "v_cvt_pkrtz_f16_f32 v40, v40, v41\n"
        "v_pk_add_f16 v38, v38, 1.0 op_sel_hi:[1,0]\n"
        "v_pk_add_f16 v40, v40, 1.0 op_sel_hi:[1,0]\n"
        "v_rcp_f16 v2, v38\n"
        "s_nop 0\n"
        "v_pk_mul_f32 v[10:11], v[80:81], v[50:51]\n"
        "v_pk_mul_f32 v[12:13], v[82:83], v[52:53]\n"
        "v_cvt_pk_bf16_f32 v92, v10, v11\n"
        "v_cvt_pk_bf16_f32 v93, v12, v13\n"
        "v_rcp_f16 v3, v40\n"
        "v_rcp_f16_sdwa v2, v38 dst_sel:WORD_1 dst_unused:UNUSED_PRESERVE src0_sel:WORD_1\n"
        "s_waitcnt lgkmcnt(0)\n"
        "v_mfma_f32_16x16x32_bf16 v[14:17], v[92:95], v[34:37], 0\n"
        "v_rcp_f16_sdwa v3, v40 dst_sel:WORD_1 dst_unused:UNUSED_PRESERVE src0_sel:WORD_1\n"
        "s_nop 1\n"
        "v_mfma_f32_16x16x16_f16 v[72:75], v[2:3], v[6:7], v[72:75]\n"
        "s_nop 7\n"
        "v_pk_mul_f32 v[18:19], v[72:73], v[62:63]\n"
        "v_pk_mul_f32 v[20:21], v[74:75], v[64:65]\n"
        "v_cvt_pk_bf16_f32 v94, v18, v19\n"
        "v_cvt_pk_bf16_f32 v95, v20, v21\n"
        "s_nop 1\n"
        "v_mfma_f32_16x16x32_bf16 v[10:13], v[92:95], v[54:57], 0\n"
        "s_nop 7\n"
        "v_add_f32_dpp v47, v12, v17 quad_perm:[0,1,2,3] row_mask:0x8 bank_mask:0xf\n"
        "v_cvt_pk_bf16_f32 v1, v46, v47\n"
        "global_store_short v8, v1, s[30:31]\n"
        "global_store_short_d16_hi v8, v1, s[30:31] offset:128\n"
        "FIN%=:\n"
        "s_endpgm\n"
        "SLOW%=:\n"
        "s_sleep 1\n"
        "ds_read_b32 v3, v10 offset:3328\n"
        "ds_read_b128 v[24:27], v10 offset:1024\n"
        "ds_read_b128 v[68:71], v10\n"
        "ds_read_b64 v[66:67], v17 offset:120\n"
        "s_waitcnt lgkmcnt(0)\n"
        "v_cmp_eq_u32 vcc, 0, v3\n"
        "s_cbranch_vccnz SLOW%=\n"
        "s_branch BACK%=\n"
        "CE%=:\n"
        :
        : "s"(__builtin_amdgcn_kernarg_segment_ptr()), "s"(blockIdx.x), "v"(threadIdx.x),
          "s"((unsigned)(size_t)(LDSP char*)smem)
        : "v1", "v2", "v3", "v4", "v5", "v6", "v7", "v8", "v9", "v10", "v11", "v12", "v13", "v14", "v15", "v16", "v17", "v18", "v19", "v20", "v21", "v22", "v23", "v24", "v25", "v26", "v27", "v28", "v29", "v30", "v31", "v32", "v33", "v34", "v35", "v36", "v37", "v38", "v39", "v40", "v41", "v42", "v43", "v44", "v45", "v46", "v47", "v48", "v49", "v50", "v51", "v52", "v53", "v54", "v55", "v56", "v57", "v58", "v59", "v60", "v61", "v62", "v63", "v64", "v65", "v66", "v67", "v68", "v69", "v70", "v71", "v72", "v73", "v74", "v75", "v76", "v77", "v78", "v79", "v80", "v81", "v82", "v83", "v84", "v85", "v86", "v87", "v88", "v89", "v90", "v91", "v92", "v93", "v94", "v95", "v96", "v97", "v98", "v99", "v100", "v101", "v102", "v103", "v104", "v105", "v106", "v107", "v108", "v109", "v110", "v111", "v112", "v113", "v114", "v115", "v116", "v117", "v118", "v119", "v120", "v121", "v122", "v123", "v124", "v125", "v126", "v127", "s16", "s17", "s18", "s19", "s20", "s21", "s22", "s23", "s24", "s25", "s26", "s27", "s28", "s29", "s30", "s31", "s32", "s33", "s34", "s35", "s36", "s37", "s38", "s39", "s40", "s41", "s42", "s43", "s44", "s45", "s46", "s47", "s48", "s49", "s50", "s51", "s52", "s53", "s54", "s55", "s56", "s57", "s58", "s59", "s60", "s61", "s62", "s63", "s64", "s65", "s66", "s67", "s68", "s69", "s70", "s71", "s72", "s73", "s74", "s75", "s76", "s77", "s78", "s79", "s80", "s81", "s82", "s83", "s84", "s85", "s86", "s87", "s88", "s89", "s90", "s91", "s92", "s93", "s94", "s95", "s96", "s97", "s98", "s99", "vcc", "memory");
}

extern "C" void kernel_launch(void* const* d_in, const int* in_sizes, int n_in, void* d_out, int out_size, void* d_ws,
                              size_t ws_size, hipStream_t stream) {
    din_kernel<<<256, 576, 0, stream>>>(d_in[0], d_in[1], d_in[2], d_in[3], d_in[4], d_in[5], d_in[6], d_out);
}
```
